```python
import torch

def _softmax(x, axis=-1):
    return torch.softmax(x, dim=axis)
def _gelu(x):
    return torch.nn.functional.gelu(x, approximate="tanh")
def _silu(x):
    return torch.nn.functional.silu(x)
def _sigmoid(x):
    return torch.sigmoid(x)
def _layernorm(x, axis=-1, eps=1e-5):
    m = x.mean(dim=axis, keepdim=True)
    v = x.var(dim=axis, keepdim=True, unbiased=False)
    return (x - m) / torch.sqrt(v + eps)
def _scatter_add(tgt, idx, src, axis=0):
    return tgt.clone().index_add_(axis, idx, src)
def _segment_sum(src, seg, n):
    out = torch.zeros((n,) + src.shape[1:], dtype=src.dtype)
    return out.index_add_(0, seg, src)
def _pad(x, pw):
    flat = tuple(v for pair in reversed(pw) for v in pair)
    return torch.nn.functional.pad(x, flat)

kernel_name = 'standalone__mha_softmax__near_ridge'

def setup_inputs():

    _w = (lambda a: (lambda t: t.to(torch.bfloat16) if t.is_floating_point() else t)(torch.as_tensor(a)))
    B, H, Sq, Sk, D = 8, 16, 384, 2048, 256
    return {
        "q": _w((lambda *s: torch.randn(*s, dtype=torch.bfloat16) * 0.8)(B, H, Sq, D)),
        "k": _w((lambda *s: torch.randn(*s, dtype=torch.bfloat16) * 0.8)(B, H, Sk, D)),
        "v": _w((lambda *s: torch.randn(*s, dtype=torch.bfloat16) * 0.05)(B, H, Sk, D)),
    }

def reference(q, k, v):
    scale = q.shape[-1] ** -0.5
    scores = torch.einsum("bhqd,bhkd->bhqk", q, k) * scale
    p = _softmax(scores, axis=-1)
    return torch.einsum("bhqk,bhkd->bhqd", p, v)
```

```cpp
#include <hip/hip_runtime.h>
#include <hip/hip_bf16.h>

constexpr int BH = 128, SQ = 384, SK = 2048, D = 256;
constexpr int KT = 32;
typedef short bf16x8 __attribute__((ext_vector_type(8)));
typedef short s16x4 __attribute__((ext_vector_type(4)));
typedef float f32x4 __attribute__((ext_vector_type(4)));
typedef unsigned u32x4 __attribute__((ext_vector_type(4)));
typedef unsigned u32x2 __attribute__((ext_vector_type(2)));
typedef float f32x2 __attribute__((ext_vector_type(2)));
typedef __bf16 bf16x2 __attribute__((ext_vector_type(2)));

__device__ __forceinline__ unsigned cvtpk(float lo, float hi) {
  f32x2 v = {lo, hi};
  bf16x2 b = __builtin_convertvector(v, bf16x2);
  return __builtin_bit_cast(unsigned, b);
}
__device__ __forceinline__ s16x4 tr_read(unsigned a) {
  s16x4 r;
  asm volatile("ds_read_b64_tr_b16 %0, %1\n\ts_waitcnt lgkmcnt(0)" : "=&v"(r) : "v"(a) : "memory");
  return r;
}

__global__ __launch_bounds__(256, 1) void attn_base(const unsigned short* __restrict__ Q, const unsigned short* __restrict__ K,
                                                    const unsigned short* __restrict__ V, unsigned short* __restrict__ O) {
  __shared__ __attribute__((aligned(16))) char lds[2 * KT * 512];
  char* Ks = lds;
  char* Vs = lds + KT * 512;
  const int tid = threadIdx.x, lane = tid & 63, wid = tid >> 6, g = lane >> 4, l16 = lane & 15;
  const int b = blockIdx.x, xcd = b & 7, i = b >> 3, head = xcd * 16 + (i >> 1), half = i & 1;
  const int row0 = half * 192 + wid * 48;
  const unsigned short* Qh = Q + ((size_t)head * SQ + row0) * D;
  const unsigned short* Kh = K + (size_t)head * SK * D;
  const unsigned short* Vh = V + (size_t)head * SK * D;
  bf16x8 qf[3][8];
#pragma unroll
  for (int rb = 0; rb < 3; ++rb)
#pragma unroll
    for (int ds = 0; ds < 8; ++ds) qf[rb][ds] = *(const bf16x8*)(Qh + (size_t)(16 * rb + l16) * D + 32 * ds + 8 * g);
  f32x4 o[16][3];
#pragma unroll
  for (int db = 0; db < 16; ++db)
#pragma unroll
    for (int rb = 0; rb < 3; ++rb) o[db][rb] = f32x4{0, 0, 0, 0};
  float m[3] = {-1e30f, -1e30f, -1e30f}, l[3] = {0, 0, 0};
  const float C = 0.0625f * 1.4426950408889634f;
  const unsigned vbase = (unsigned)(size_t)Vs;
  for (int t = 0; t < SK / KT; ++t) {
    __syncthreads();
#pragma unroll
    for (int j = 0; j < 4; ++j) {
      const int n = tid + 256 * j, r = n >> 5, c = n & 31;
      const u32x4 kv = *(const u32x4*)(Kh + (size_t)(t * KT + r) * D + c * 8);
      const u32x4 vv = *(const u32x4*)(Vh + (size_t)(t * KT + r) * D + c * 8);
      *(u32x4*)(Ks + r * 512 + 16 * (c ^ (r & 15))) = kv;
      *(u32x4*)(Vs + r * 512 + 16 * (c ^ ((r & 7) << 1))) = vv;
    }
    __syncthreads();
    f32x4 s[2][3];
#pragma unroll
    for (int kb = 0; kb < 2; ++kb)
#pragma unroll
      for (int rb = 0; rb < 3; ++rb) s[kb][rb] = f32x4{0, 0, 0, 0};
#pragma unroll
    for (int kb = 0; kb < 2; ++kb)
#pragma unroll
      for (int ds = 0; ds < 8; ++ds) {
        const bf16x8 kf = *(const bf16x8*)(Ks + (16 * kb + l16) * 512 + 16 * ((4 * ds + g) ^ l16));
#pragma unroll
        for (int rb = 0; rb < 3; ++rb) s[kb][rb] = __builtin_amdgcn_mfma_f32_16x16x32_bf16(kf, qf[rb][ds], s[kb][rb], 0, 0, 0);
      }
    bf16x8 pf[3];
#pragma unroll
    for (int rb = 0; rb < 3; ++rb) {
      float mx = fmaxf(fmaxf(fmaxf(s[0][rb][0], s[0][rb][1]), fmaxf(s[0][rb][2], s[0][rb][3])),
                       fmaxf(fmaxf(s[1][rb][0], s[1][rb][1]), fmaxf(s[1][rb][2], s[1][rb][3])));
      mx = fmaxf(mx, __shfl_xor(mx, 16));
      mx = fmaxf(mx, __shfl_xor(mx, 32));
      const float mn = fmaxf(m[rb], mx);
      const float alpha = __builtin_amdgcn_exp2f((m[rb] - mn) * C);
      m[rb] = mn;
      float p[8], sum = 0;
#pragma unroll
      for (int j = 0; j < 8; ++j) { p[j] = __builtin_amdgcn_exp2f((s[j >> 2][rb][j & 3] - mn) * C); sum += p[j]; }
      l[rb] = l[rb] * alpha + sum;
      u32x4 w = {cvtpk(p[0], p[1]), cvtpk(p[2], p[3]), cvtpk(p[4], p[5]), cvtpk(p[6], p[7])};
      pf[rb] = __builtin_bit_cast(bf16x8, w);
#pragma unroll
      for (int db = 0; db < 16; ++db) o[db][rb] *= alpha;
    }
#pragma unroll
    for (int db = 0; db < 16; ++db) {
      const int q = l16 >> 2, p = l16 & 3;
      const int r0 = 4 * g + q, r1 = 16 + r0;
      const s16x4 lo = tr_read(vbase + r0 * 512 + 16 * ((2 * db + (p >> 1)) ^ ((r0 & 7) << 1)) + 8 * (p & 1));
      const s16x4 hi = tr_read(vbase + r1 * 512 + 16 * ((2 * db + (p >> 1)) ^ ((r1 & 7) << 1)) + 8 * (p & 1));
      const bf16x8 vf = __builtin_shufflevector(lo, hi, 0, 1, 2, 3, 4, 5, 6, 7);
#pragma unroll
      for (int rb = 0; rb < 3; ++rb) o[db][rb] = __builtin_amdgcn_mfma_f32_16x16x32_bf16(vf, pf[rb], o[db][rb], 0, 0, 0);
    }
  }
#pragma unroll
  for (int rb = 0; rb < 3; ++rb) {
    float ls = l[rb];
    ls += __shfl_xor(ls, 16);
    ls += __shfl_xor(ls, 32);
    const float il = 1.0f / ls;
    unsigned short* Or = O + ((size_t)head * SQ + row0 + 16 * rb + l16) * D + 4 * g;
#pragma unroll
    for (int db = 0; db < 16; ++db) {
      u32x2 w = {cvtpk(o[db][rb][0] * il, o[db][rb][1] * il), cvtpk(o[db][rb][2] * il, o[db][rb][3] * il)};
      *(u32x2*)(Or + 16 * db) = w;
    }
  }
}

extern "C" void kernel_launch(void* const* d_in, const int* in_sizes, int n_in, void* d_out, int out_size, void* d_ws, size_t ws_size,
                              hipStream_t stream) {
  attn_base<<<256, 256, 0, stream>>>((const unsigned short*)d_in[0], (const unsigned short*)d_in[1], (const unsigned short*)d_in[2],
                                     (unsigned short*)d_out);
}
```

```cpp
#include <hip/hip_runtime.h>
#include <hip/hip_bf16.h>

__global__ __launch_bounds__(256, 1) void attn_fwd(const char* __restrict__ Q, const char* __restrict__ K, const char* __restrict__ V,
                                                   char* __restrict__ O, char* __restrict__ ws) {
  __shared__ __attribute__((aligned(16))) char lds[163840];
  unsigned tid = threadIdx.x;
  asm volatile("" :: "s"((unsigned)(size_t)lds));
  asm volatile(
    "s_load_dwordx8 s[72:79], %[ka], 0x0\n"
    "s_load_dwordx2 s[80:81], %[ka], 0x20\n"
    "v_readfirstlane_b32 s85, %[tid]\n"
    "v_and_b32_e32 v0, 63, %[tid]\n"
    "s_lshr_b32 s85, s85, 6\n"
    "s_and_b32 s82, %[bid], 7\n"
    "s_lshl_b32 s82, s82, 4\n"
    "s_lshr_b32 s83, %[bid], 4\n"
    "s_add_u32 s82, s82, s83\n"
    "s_bfe_u32 s83, %[bid], 0x10003\n"
    "s_mul_i32 s83, s83, 192\n"
    "s_mul_i32 s84, s82, 384\n"
    "s_add_u32 s83, s83, s84\n"
    "s_mul_i32 s84, s85, 48\n"
    "s_add_u32 s83, s83, s84\n"
    "s_lshl_b32 s83, s83, 9\n"
    "s_lshl_b32 s82, s82, 20\n"
    "s_mov_b32 s38, 0x100000\n"
    "s_mov_b32 s39, 0x20000\n"
    "s_mov_b32 s46, 0x6000\n"
    "s_mov_b32 s47, 0x20000\n"
    "s_mov_b32 s42, 0x100000\n"
    "s_mov_b32 s43, 0x20000\n"
    "s_mov_b32 s50, 0x6000\n"
    "s_mov_b32 s51, 0x20000\n"
    "s_lshl_b32 s54, s85, 13\n"
    "s_add_u32 s55, s54, 3968\n"
    "v_lshrrev_b32_e32 v1, 2, v0\n"
    "v_and_b32_e32 v1, 7, v1\n"
    "v_lshrrev_b32_e32 v2, 2, v1\n"
    "v_lshlrev_b32_e32 v3, 1, v2\n"
    "v_and_b32_e32 v4, 3, v0\n"
    "v_xor_b32_e32 v3, v3, v4\n"
    "v_lshrrev_b32_e32 v4, 5, v0\n"
    "v_xor_b32_e32 v6, v3, v4\n"
    "v_lshlrev_b32_e32 v4, 6, v4\n"
    "v_lshl_add_u32 v3, v3, 4, v4\n"
    "v_lshl_add_u32 v6, v6, 4, v4\n"
    "v_and_b32_e32 v4, 3, v1\n"
    "s_and_b32 s58, s85, 1\n"
    "s_lshr_b32 s59, s85, 1\n"
    "s_lshl_b32 s59, s59, 8\n"
    "v_lshl_add_u32 v5, v1, 12, v3\n"
    "s_lshl_b32 s56, s58, 9\n"
    "s_add_u32 s56, s56, s59\n"
    "v_add_u32_e32 v245, s56, v5\n"
    "v_lshl_add_u32 v5, v4, 12, v6\n"
    "v_lshl_add_u32 v5, v2, 9, v5\n"
    "s_lshl_b32 s56, s58, 14\n"
    "s_add_u32 s56, s56, s59\n"
    "v_add_u32_e32 v246, s56, v5\n"
    "v_lshl_add_u32 v5, v4, 9, v3\n"
    "v_lshl_add_u32 v251, v2, 12, v5\n"
    "s_mov_b32 s52, 0\n"
    "s_mov_b32 s53, 0\n"
    "s_waitcnt lgkmcnt(0)\n"
    "s_add_u32 s36, s74, s82\n"
    "s_addc_u32 s37, s75, 0\n"
    "s_and_b32 s37, s37, 0xffff\n"
    "s_add_u32 s44, s72, s83\n"
    "s_addc_u32 s45, s73, 0\n"
    "s_and_b32 s45, s45, 0xffff\n"
    "s_add_u32 s40, s76, s82\n"
    "s_addc_u32 s41, s77, 0\n"
    "s_and_b32 s41, s41, 0xffff\n"
    "s_add_u32 s48, s78, s83\n"
    "s_addc_u32 s49, s79, 0\n"
    "s_and_b32 s49, s49, 0xffff\n"
    "s_add_u32 m0, s54, 65536\n"
    "s_mov_b32 s59, 0\n"
    "buffer_load_dwordx4 v251, s[44:47], s59 offen offset:0 nt lds\n"
    "s_add_u32 m0, s54, 66432\n"
    "s_mov_b32 s59, 0\n"
    "buffer_load_dwordx4 v251, s[44:47], s59 offen offset:128 nt lds\n"
    "s_add_u32 m0, s54, 67328\n"
    "s_mov_b32 s59, 0\n"
    "buffer_load_dwordx4 v251, s[44:47], s59 offen offset:256 nt lds\n"
    "s_add_u32 m0, s54, 68224\n"
    "s_mov_b32 s59, 0\n"
    "buffer_load_dwordx4 v251, s[44:47], s59 offen offset:384 nt lds\n"
    "s_add_u32 m0, s54, 69632\n"
    "s_mov_b32 s59, 2048\n"
    "buffer_load_dwordx4 v251, s[44:47], s59 offen offset:0 nt lds\n"
    "s_add_u32 m0, s54, 70528\n"
    "s_mov_b32 s59, 2048\n"
    "buffer_load_dwordx4 v251, s[44:47], s59 offen offset:128 nt lds\n"
    "s_add_u32 m0, s54, 71424\n"
    "s_mov_b32 s59, 2048\n"
    "buffer_load_dwordx4 v251, s[44:47], s59 offen offset:256 nt lds\n"
    "s_add_u32 m0, s54, 72320\n"
    "s_mov_b32 s59, 2048\n"
    "buffer_load_dwordx4 v251, s[44:47], s59 offen offset:384 nt lds\n"
    "s_add_u32 m0, s54, 0\n"
    "s_nop 0\n"
    "buffer_load_dwordx4 v245, s[36:39], s52 offen offset:0 lds\n"
    "buffer_load_dwordx4 v245, s[36:39], s52 offen offset:1024 lds\n"
    "buffer_load_dwordx4 v245, s[36:39], s52 offen offset:2048 lds\n"
    "buffer_load_dwordx4 v245, s[36:39], s52 offen offset:3072 lds\n"
    "s_add_u32 m0, s55, 0\n"
    "s_nop 0\n"
    "buffer_load_dwordx4 v245, s[36:39], s52 offen offset:128 lds\n"
    "buffer_load_dwordx4 v245, s[36:39], s52 offen offset:1152 lds\n"
    "buffer_load_dwordx4 v245, s[36:39], s52 offen offset:2176 lds\n"
    "buffer_load_dwordx4 v245, s[36:39], s52 offen offset:3200 lds\n"
    "s_add_u32 s52, s52, 32768\n"
    "v_and_b32_e32 v1, 15, v0\n"
    "v_lshrrev_b32_e32 v2, 4, v0\n"
    "v_lshrrev_b32_e32 v3, 3, v1\n"
    "v_lshlrev_b32_e32 v4, 1, v3\n"
    "v_xor_b32_e32 v4, v4, v2\n"
    "v_lshlrev_b32_e32 v4, 4, v4\n"
    "v_lshl_add_u32 v4, v3, 8, v4\n"
    "v_and_b32_e32 v5, 3, v1\n"
    "v_lshl_add_u32 v4, v5, 6, v4\n"
    "v_bfe_u32 v5, v1, 2, 1\n"
    "v_lshl_add_u32 v240, v5, 13, v4\n"
    "v_lshl_add_u32 v6, v5, 12, v4\n"
    "v_add_u32_e32 v6, s54, v6\n"
    "v_add_u32_e32 v252, 65536, v6\n"
    "v_add_u32_e32 v253, 131072, v6\n"
    "v_lshrrev_b32_e32 v3, 1, v2\n"
    "v_and_b32_e32 v4, 1, v2\n"
    "v_lshlrev_b32_e32 v5, 13, v3\n"
    "v_lshl_add_u32 v5, v4, 8, v5\n"
    "v_lshrrev_b32_e32 v7, 2, v1\n"
    "v_lshl_add_u32 v5, v7, 6, v5\n"
    "v_bfe_u32 v7, v1, 1, 1\n"
    "v_lshl_add_u32 v5, v7, 9, v5\n"
    "v_lshl_add_u32 v7, v4, 1, v7\n"
    "v_lshl_add_u32 v5, v7, 4, v5\n"
    "v_and_b32_e32 v7, 1, v1\n"
    "v_lshl_add_u32 v5, v7, 3, v5\n"
    "v_add_u32_e32 v241, 65536, v5\n"
    "v_xor_b32_e32 v242, 16, v241\n"
    "v_xor_b32_e32 v243, 32, v241\n"
    "v_xor_b32_e32 v244, 48, v241\n"
    "s_add_u32 s58, s54, 131072\n"
    "v_lshl_add_u32 v247, v0, 2, s58\n"
    "v_and_b32_e32 v7, 31, v0\n"
    "v_lshlrev_b32_e32 v7, 2, v7\n"
    "v_lshl_add_u32 v7, v2, 13, v7\n"
    "v_add_u32_e32 v248, 131072, v7\n"
    "v_mov_b32_e32 v250, 0\n"
    "s_mov_b32 s57, 0x45800000\n"
    "s_mov_b32 s60, 0x3db8aa3b\n"
    "s_mov_b32 s61, 0x3db8aa3b\n"
    "s_mov_b32 s56, 16\n"
    "v_accvgpr_write_b32 a16, 0\n"
    "v_accvgpr_write_b32 a17, 0\n"
    "v_accvgpr_write_b32 a18, 0\n"
    "v_accvgpr_write_b32 a19, 0\n"
    "v_accvgpr_write_b32 a20, 0\n"
    "v_accvgpr_write_b32 a21, 0\n"
    "v_accvgpr_write_b32 a22, 0\n"
    "v_accvgpr_write_b32 a23, 0\n"
    "v_accvgpr_write_b32 a24, 0\n"
    "v_accvgpr_write_b32 a25, 0\n"
    "v_accvgpr_write_b32 a26, 0\n"
    "v_accvgpr_write_b32 a27, 0\n"
    "v_accvgpr_write_b32 a28, 0\n"
    "v_accvgpr_write_b32 a29, 0\n"
    "v_accvgpr_write_b32 a30, 0\n"
    "v_accvgpr_write_b32 a31, 0\n"
    "v_accvgpr_write_b32 a32, 0\n"
    "v_accvgpr_write_b32 a33, 0\n"
    "v_accvgpr_write_b32 a34, 0\n"
    "v_accvgpr_write_b32 a35, 0\n"
    "v_accvgpr_write_b32 a36, 0\n"
    "v_accvgpr_write_b32 a37, 0\n"
    "v_accvgpr_write_b32 a38, 0\n"
    "v_accvgpr_write_b32 a39, 0\n"
    "v_accvgpr_write_b32 a40, 0\n"
    "v_accvgpr_write_b32 a41, 0\n"
    "v_accvgpr_write_b32 a42, 0\n"
    "v_accvgpr_write_b32 a43, 0\n"
    "v_accvgpr_write_b32 a44, 0\n"
    "v_accvgpr_write_b32 a45, 0\n"
    "v_accvgpr_write_b32 a46, 0\n"
    "v_accvgpr_write_b32 a47, 0\n"
    "v_accvgpr_write_b32 a48, 0\n"
    "v_accvgpr_write_b32 a49, 0\n"
    "v_accvgpr_write_b32 a50, 0\n"
    "v_accvgpr_write_b32 a51, 0\n"
    "v_accvgpr_write_b32 a52, 0\n"
    "v_accvgpr_write_b32 a53, 0\n"
    "v_accvgpr_write_b32 a54, 0\n"
    "v_accvgpr_write_b32 a55, 0\n"
    "v_accvgpr_write_b32 a56, 0\n"
    "v_accvgpr_write_b32 a57, 0\n"
    "v_accvgpr_write_b32 a58, 0\n"
    "v_accvgpr_write_b32 a59, 0\n"
    "v_accvgpr_write_b32 a60, 0\n"
    "v_accvgpr_write_b32 a61, 0\n"
    "v_accvgpr_write_b32 a62, 0\n"
    "v_accvgpr_write_b32 a63, 0\n"
    "v_accvgpr_write_b32 a64, 0\n"
    "v_accvgpr_write_b32 a65, 0\n"
    "v_accvgpr_write_b32 a66, 0\n"
    "v_accvgpr_write_b32 a67, 0\n"
    "v_accvgpr_write_b32 a68, 0\n"
    "v_accvgpr_write_b32 a69, 0\n"
    "v_accvgpr_write_b32 a70, 0\n"
    "v_accvgpr_write_b32 a71, 0\n"
    "v_accvgpr_write_b32 a72, 0\n"
    "v_accvgpr_write_b32 a73, 0\n"
    "v_accvgpr_write_b32 a74, 0\n"
    "v_accvgpr_write_b32 a75, 0\n"
    "v_accvgpr_write_b32 a76, 0\n"
    "v_accvgpr_write_b32 a77, 0\n"
    "v_accvgpr_write_b32 a78, 0\n"
    "v_accvgpr_write_b32 a79, 0\n"
    "v_accvgpr_write_b32 a80, 0\n"
    "v_accvgpr_write_b32 a81, 0\n"
    "v_accvgpr_write_b32 a82, 0\n"
    "v_accvgpr_write_b32 a83, 0\n"
    "v_accvgpr_write_b32 a84, 0\n"
    "v_accvgpr_write_b32 a85, 0\n"
    "v_accvgpr_write_b32 a86, 0\n"
    "v_accvgpr_write_b32 a87, 0\n"
    "v_accvgpr_write_b32 a88, 0\n"
    "v_accvgpr_write_b32 a89, 0\n"
    "v_accvgpr_write_b32 a90, 0\n"
    "v_accvgpr_write_b32 a91, 0\n"
    "v_accvgpr_write_b32 a92, 0\n"
    "v_accvgpr_write_b32 a93, 0\n"
    "v_accvgpr_write_b32 a94, 0\n"
    "v_accvgpr_write_b32 a95, 0\n"
    "v_accvgpr_write_b32 a96, 0\n"
    "v_accvgpr_write_b32 a97, 0\n"
    "v_accvgpr_write_b32 a98, 0\n"
    "v_accvgpr_write_b32 a99, 0\n"
    "v_accvgpr_write_b32 a100, 0\n"
    "v_accvgpr_write_b32 a101, 0\n"
    "v_accvgpr_write_b32 a102, 0\n"
    "v_accvgpr_write_b32 a103, 0\n"
    "v_accvgpr_write_b32 a104, 0\n"
    "v_accvgpr_write_b32 a105, 0\n"
    "v_accvgpr_write_b32 a106, 0\n"
    "v_accvgpr_write_b32 a107, 0\n"
    "v_accvgpr_write_b32 a108, 0\n"
    "v_accvgpr_write_b32 a109, 0\n"
    "v_accvgpr_write_b32 a110, 0\n"
    "v_accvgpr_write_b32 a111, 0\n"
    "v_accvgpr_write_b32 a112, 0\n"
    "v_accvgpr_write_b32 a113, 0\n"
    "v_accvgpr_write_b32 a114, 0\n"
    "v_accvgpr_write_b32 a115, 0\n"
    "v_accvgpr_write_b32 a116, 0\n"
    "v_accvgpr_write_b32 a117, 0\n"
    "v_accvgpr_write_b32 a118, 0\n"
    "v_accvgpr_write_b32 a119, 0\n"
    "v_accvgpr_write_b32 a120, 0\n"
    "v_accvgpr_write_b32 a121, 0\n"
    "v_accvgpr_write_b32 a122, 0\n"
    "v_accvgpr_write_b32 a123, 0\n"
    "v_accvgpr_write_b32 a124, 0\n"
    "v_accvgpr_write_b32 a125, 0\n"
    "v_accvgpr_write_b32 a126, 0\n"
    "v_accvgpr_write_b32 a127, 0\n"
    "v_accvgpr_write_b32 a128, 0\n"
    "v_accvgpr_write_b32 a129, 0\n"
    "v_accvgpr_write_b32 a130, 0\n"
    "v_accvgpr_write_b32 a131, 0\n"
    "v_accvgpr_write_b32 a132, 0\n"
    "v_accvgpr_write_b32 a133, 0\n"
    "v_accvgpr_write_b32 a134, 0\n"
    "v_accvgpr_write_b32 a135, 0\n"
    "v_accvgpr_write_b32 a136, 0\n"
    "v_accvgpr_write_b32 a137, 0\n"
    "v_accvgpr_write_b32 a138, 0\n"
    "v_accvgpr_write_b32 a139, 0\n"
    "v_accvgpr_write_b32 a140, 0\n"
    "v_accvgpr_write_b32 a141, 0\n"
    "v_accvgpr_write_b32 a142, 0\n"
    "v_accvgpr_write_b32 a143, 0\n"
    "v_accvgpr_write_b32 a144, 0\n"
    "v_accvgpr_write_b32 a145, 0\n"
    "v_accvgpr_write_b32 a146, 0\n"
    "v_accvgpr_write_b32 a147, 0\n"
    "v_accvgpr_write_b32 a148, 0\n"
    "v_accvgpr_write_b32 a149, 0\n"
    "v_accvgpr_write_b32 a150, 0\n"
    "v_accvgpr_write_b32 a151, 0\n"
    "v_accvgpr_write_b32 a152, 0\n"
    "v_accvgpr_write_b32 a153, 0\n"
    "v_accvgpr_write_b32 a154, 0\n"
    "v_accvgpr_write_b32 a155, 0\n"
    "v_accvgpr_write_b32 a156, 0\n"
    "v_accvgpr_write_b32 a157, 0\n"
    "v_accvgpr_write_b32 a158, 0\n"
    "v_accvgpr_write_b32 a159, 0\n"
    "v_accvgpr_write_b32 a160, 0\n"
    "v_accvgpr_write_b32 a161, 0\n"
    "v_accvgpr_write_b32 a162, 0\n"
    "v_accvgpr_write_b32 a163, 0\n"
    "v_accvgpr_write_b32 a164, 0\n"
    "v_accvgpr_write_b32 a165, 0\n"
    "v_accvgpr_write_b32 a166, 0\n"
    "v_accvgpr_write_b32 a167, 0\n"
    "v_accvgpr_write_b32 a168, 0\n"
    "v_accvgpr_write_b32 a169, 0\n"
    "v_accvgpr_write_b32 a170, 0\n"
    "v_accvgpr_write_b32 a171, 0\n"
    "v_accvgpr_write_b32 a172, 0\n"
    "v_accvgpr_write_b32 a173, 0\n"
    "v_accvgpr_write_b32 a174, 0\n"
    "v_accvgpr_write_b32 a175, 0\n"
    "v_accvgpr_write_b32 a176, 0\n"
    "v_accvgpr_write_b32 a177, 0\n"
    "v_accvgpr_write_b32 a178, 0\n"
    "v_accvgpr_write_b32 a179, 0\n"
    "v_accvgpr_write_b32 a180, 0\n"
    "v_accvgpr_write_b32 a181, 0\n"
    "v_accvgpr_write_b32 a182, 0\n"
    "v_accvgpr_write_b32 a183, 0\n"
    "v_accvgpr_write_b32 a184, 0\n"
    "v_accvgpr_write_b32 a185, 0\n"
    "v_accvgpr_write_b32 a186, 0\n"
    "v_accvgpr_write_b32 a187, 0\n"
    "v_accvgpr_write_b32 a188, 0\n"
    "v_accvgpr_write_b32 a189, 0\n"
    "v_accvgpr_write_b32 a190, 0\n"
    "v_accvgpr_write_b32 a191, 0\n"
    "v_mov_b32_e32 v237, 0\n"
    "v_mov_b32_e32 v238, 0\n"
    "v_mov_b32_e32 v239, 0\n"
    "s_waitcnt vmcnt(8)\n"
    "ds_read_b128 v[120:123], v252 offset:0\n"
    "ds_read_b128 v[124:127], v252 offset:512\n"
    "ds_read_b128 v[128:131], v252 offset:1024\n"
    "ds_read_b128 v[132:135], v252 offset:1536\n"
    "ds_read_b128 v[136:139], v252 offset:2048\n"
    "ds_read_b128 v[140:143], v252 offset:2560\n"
    "ds_read_b128 v[144:147], v252 offset:3072\n"
    "ds_read_b128 v[148:151], v252 offset:3584\n"
    "s_waitcnt lgkmcnt(0)\n"
    "s_add_u32 m0, s54, 98304\n"
    "s_mov_b32 s59, 8192\n"
    "buffer_load_dwordx4 v251, s[44:47], s59 offen offset:0 nt lds\n"
    "v_lshlrev_b32_e32 v228, 16, v120\n"
    "v_and_b32_e32 v229, 0xffff0000, v120\n"
    "v_lshlrev_b32_e32 v230, 16, v121\n"
    "v_and_b32_e32 v231, 0xffff0000, v121\n"
    "v_pk_mul_f32 v[228:229], v[228:229], s[60:61]\n"
    "v_pk_mul_f32 v[230:231], v[230:231], s[60:61]\n"
    "v_cvt_pk_bf16_f32 v120, v228, v229\n"
    "v_cvt_pk_bf16_f32 v121, v230, v231\n"
    "v_lshlrev_b32_e32 v228, 16, v122\n"
    "v_and_b32_e32 v229, 0xffff0000, v122\n"
    "v_lshlrev_b32_e32 v230, 16, v123\n"
    "v_and_b32_e32 v231, 0xffff0000, v123\n"
    "v_pk_mul_f32 v[228:229], v[228:229], s[60:61]\n"
    "v_pk_mul_f32 v[230:231], v[230:231], s[60:61]\n"
    "v_cvt_pk_bf16_f32 v122, v228, v229\n"
    "v_cvt_pk_bf16_f32 v123, v230, v231\n"
    "s_add_u32 m0, s54, 99200\n"
    "s_mov_b32 s59, 8192\n"
    "buffer_load_dwordx4 v251, s[44:47], s59 offen offset:128 nt lds\n"
    "v_lshlrev_b32_e32 v228, 16, v124\n"
    "v_and_b32_e32 v229, 0xffff0000, v124\n"
    "v_lshlrev_b32_e32 v230, 16, v125\n"
    "v_and_b32_e32 v231, 0xffff0000, v125\n"
    "v_pk_mul_f32 v[228:229], v[228:229], s[60:61]\n"
    "v_pk_mul_f32 v[230:231], v[230:231], s[60:61]\n"
    "v_cvt_pk_bf16_f32 v124, v228, v229\n"
    "v_cvt_pk_bf16_f32 v125, v230, v231\n"
    "v_lshlrev_b32_e32 v228, 16, v126\n"
    "v_and_b32_e32 v229, 0xffff0000, v126\n"
    "v_lshlrev_b32_e32 v230, 16, v127\n"
    "v_and_b32_e32 v231, 0xffff0000, v127\n"
    "v_pk_mul_f32 v[228:229], v[228:229], s[60:61]\n"
    "v_pk_mul_f32 v[230:231], v[230:231], s[60:61]\n"
    "v_cvt_pk_bf16_f32 v126, v228, v229\n"
    "v_cvt_pk_bf16_f32 v127, v230, v231\n"
    "s_add_u32 m0, s54, 100096\n"
    "s_mov_b32 s59, 8192\n"
    "buffer_load_dwordx4 v251, s[44:47], s59 offen offset:256 nt lds\n"
    "v_lshlrev_b32_e32 v228, 16, v128\n"
    "v_and_b32_e32 v229, 0xffff0000, v128\n"
    "v_lshlrev_b32_e32 v230, 16, v129\n"
    "v_and_b32_e32 v231, 0xffff0000, v129\n"
    "v_pk_mul_f32 v[228:229], v[228:229], s[60:61]\n"
    "v_pk_mul_f32 v[230:231], v[230:231], s[60:61]\n"
    "v_cvt_pk_bf16_f32 v128, v228, v229\n"
    "v_cvt_pk_bf16_f32 v129, v230, v231\n"
    "v_lshlrev_b32_e32 v228, 16, v130\n"
    "v_and_b32_e32 v229, 0xffff0000, v130\n"
    "v_lshlrev_b32_e32 v230, 16, v131\n"
    "v_and_b32_e32 v231, 0xffff0000, v131\n"
    "v_pk_mul_f32 v[228:229], v[228:229], s[60:61]\n"
    "v_pk_mul_f32 v[230:231], v[230:231], s[60:61]\n"
    "v_cvt_pk_bf16_f32 v130, v228, v229\n"
    "v_cvt_pk_bf16_f32 v131, v230, v231\n"
    "s_add_u32 m0, s54, 100992\n"
    "s_mov_b32 s59, 8192\n"
    "buffer_load_dwordx4 v251, s[44:47], s59 offen offset:384 nt lds\n"
    "v_lshlrev_b32_e32 v228, 16, v132\n"
    "v_and_b32_e32 v229, 0xffff0000, v132\n"
    "v_lshlrev_b32_e32 v230, 16, v133\n"
    "v_and_b32_e32 v231, 0xffff0000, v133\n"
    "v_pk_mul_f32 v[228:229], v[228:229], s[60:61]\n"
    "v_pk_mul_f32 v[230:231], v[230:231], s[60:61]\n"
    "v_cvt_pk_bf16_f32 v132, v228, v229\n"
    "v_cvt_pk_bf16_f32 v133, v230, v231\n"
    "v_lshlrev_b32_e32 v228, 16, v134\n"
    "v_and_b32_e32 v229, 0xffff0000, v134\n"
    "v_lshlrev_b32_e32 v230, 16, v135\n"
    "v_and_b32_e32 v231, 0xffff0000, v135\n"
    "v_pk_mul_f32 v[228:229], v[228:229], s[60:61]\n"
    "v_pk_mul_f32 v[230:231], v[230:231], s[60:61]\n"
    "v_cvt_pk_bf16_f32 v134, v228, v229\n"
    "v_cvt_pk_bf16_f32 v135, v230, v231\n"
    "s_add_u32 m0, s54, 102400\n"
    "s_mov_b32 s59, 10240\n"
    "buffer_load_dwordx4 v251, s[44:47], s59 offen offset:0 nt lds\n"
    "v_lshlrev_b32_e32 v228, 16, v136\n"
    "v_and_b32_e32 v229, 0xffff0000, v136\n"
    "v_lshlrev_b32_e32 v230, 16, v137\n"
    "v_and_b32_e32 v231, 0xffff0000, v137\n"
    "v_pk_mul_f32 v[228:229], v[228:229], s[60:61]\n"
    "v_pk_mul_f32 v[230:231], v[230:231], s[60:61]\n"
    "v_cvt_pk_bf16_f32 v136, v228, v229\n"
    "v_cvt_pk_bf16_f32 v137, v230, v231\n"
    "v_lshlrev_b32_e32 v228, 16, v138\n"
    "v_and_b32_e32 v229, 0xffff0000, v138\n"
    "v_lshlrev_b32_e32 v230, 16, v139\n"
    "v_and_b32_e32 v231, 0xffff0000, v139\n"
    "v_pk_mul_f32 v[228:229], v[228:229], s[60:61]\n"
    "v_pk_mul_f32 v[230:231], v[230:231], s[60:61]\n"
    "v_cvt_pk_bf16_f32 v138, v228, v229\n"
    "v_cvt_pk_bf16_f32 v139, v230, v231\n"
    "s_add_u32 m0, s54, 103296\n"
    "s_mov_b32 s59, 10240\n"
    "buffer_load_dwordx4 v251, s[44:47], s59 offen offset:128 nt lds\n"
    "v_lshlrev_b32_e32 v228, 16, v140\n"
    "v_and_b32_e32 v229, 0xffff0000, v140\n"
    "v_lshlrev_b32_e32 v230, 16, v141\n"
    "v_and_b32_e32 v231, 0xffff0000, v141\n"
    "v_pk_mul_f32 v[228:229], v[228:229], s[60:61]\n"
    "v_pk_mul_f32 v[230:231], v[230:231], s[60:61]\n"
    "v_cvt_pk_bf16_f32 v140, v228, v229\n"
    "v_cvt_pk_bf16_f32 v141, v230, v231\n"
    "v_lshlrev_b32_e32 v228, 16, v142\n"
    "v_and_b32_e32 v229, 0xffff0000, v142\n"
    "v_lshlrev_b32_e32 v230, 16, v143\n"
    "v_and_b32_e32 v231, 0xffff0000, v143\n"
    "v_pk_mul_f32 v[228:229], v[228:229], s[60:61]\n"
    "v_pk_mul_f32 v[230:231], v[230:231], s[60:61]\n"
    "v_cvt_pk_bf16_f32 v142, v228, v229\n"
    "v_cvt_pk_bf16_f32 v143, v230, v231\n"
    "s_add_u32 m0, s54, 104192\n"
    "s_mov_b32 s59, 10240\n"
    "buffer_load_dwordx4 v251, s[44:47], s59 offen offset:256 nt lds\n"
    "v_lshlrev_b32_e32 v228, 16, v144\n"
    "v_and_b32_e32 v229, 0xffff0000, v144\n"
    "v_lshlrev_b32_e32 v230, 16, v145\n"
    "v_and_b32_e32 v231, 0xffff0000, v145\n"
    "v_pk_mul_f32 v[228:229], v[228:229], s[60:61]\n"
    "v_pk_mul_f32 v[230:231], v[230:231], s[60:61]\n"
    "v_cvt_pk_bf16_f32 v144, v228, v229\n"
    "v_cvt_pk_bf16_f32 v145, v230, v231\n"
    "v_lshlrev_b32_e32 v228, 16, v146\n"
    "v_and_b32_e32 v229, 0xffff0000, v146\n"
    "v_lshlrev_b32_e32 v230, 16, v147\n"
    "v_and_b32_e32 v231, 0xffff0000, v147\n"
    "v_pk_mul_f32 v[228:229], v[228:229], s[60:61]\n"
    "v_pk_mul_f32 v[230:231], v[230:231], s[60:61]\n"
    "v_cvt_pk_bf16_f32 v146, v228, v229\n"
    "v_cvt_pk_bf16_f32 v147, v230, v231\n"
    "s_add_u32 m0, s54, 105088\n"
    "s_mov_b32 s59, 10240\n"
    "buffer_load_dwordx4 v251, s[44:47], s59 offen offset:384 nt lds\n"
    "v_lshlrev_b32_e32 v228, 16, v148\n"
    "v_and_b32_e32 v229, 0xffff0000, v148\n"
    "v_lshlrev_b32_e32 v230, 16, v149\n"
    "v_and_b32_e32 v231, 0xffff0000, v149\n"
    "v_pk_mul_f32 v[228:229], v[228:229], s[60:61]\n"
    "v_pk_mul_f32 v[230:231], v[230:231], s[60:61]\n"
    "v_cvt_pk_bf16_f32 v148, v228, v229\n"
    "v_cvt_pk_bf16_f32 v149, v230, v231\n"
    "v_lshlrev_b32_e32 v228, 16, v150\n"
    "v_and_b32_e32 v229, 0xffff0000, v150\n"
    "v_lshlrev_b32_e32 v230, 16, v151\n"
    "v_and_b32_e32 v231, 0xffff0000, v151\n"
    "v_pk_mul_f32 v[228:229], v[228:229], s[60:61]\n"
    "v_pk_mul_f32 v[230:231], v[230:231], s[60:61]\n"
    "v_cvt_pk_bf16_f32 v150, v228, v229\n"
    "v_cvt_pk_bf16_f32 v151, v230, v231\n"
    "s_waitcnt vmcnt(8)\n"
    "s_barrier\n"
    "s_add_u32 m0, s54, 131072\n"
    "s_mov_b32 s59, 16384\n"
    "buffer_load_dwordx4 v251, s[44:47], s59 offen offset:0 nt lds\n"
    "ds_read_b128 a[192:195], v240 offset:0\n"
    "ds_read_b128 a[196:199], v240 offset:512\n"
    "ds_read_b128 a[200:203], v240 offset:4096\n"
    "ds_read_b128 a[204:207], v240 offset:4608\n"
    "s_add_u32 m0, s54, 131968\n"
    "s_mov_b32 s59, 16384\n"
    "buffer_load_dwordx4 v251, s[44:47], s59 offen offset:128 nt lds\n"
    "ds_read_b128 a[208:211], v240 offset:16384\n"
    "ds_read_b128 a[212:215], v240 offset:16896\n"
    "ds_read_b128 a[216:219], v240 offset:20480\n"
    "ds_read_b128 a[220:223], v240 offset:20992\n"
    "s_add_u32 m0, s54, 132864\n"
    "s_mov_b32 s59, 16384\n"
    "buffer_load_dwordx4 v251, s[44:47], s59 offen offset:256 nt lds\n"
    "ds_read_b128 a[224:227], v240 offset:1024\n"
    "ds_read_b128 a[228:231], v240 offset:1536\n"
    "ds_read_b128 a[232:235], v240 offset:5120\n"
    "ds_read_b128 a[236:239], v240 offset:5632\n"
    "s_add_u32 m0, s54, 133760\n"
    "s_mov_b32 s59, 16384\n"
    "buffer_load_dwordx4 v251, s[44:47], s59 offen offset:384 nt lds\n"
    "ds_read_b128 a[240:243], v240 offset:17408\n"
    "ds_read_b128 a[244:247], v240 offset:17920\n"
    "ds_read_b128 a[248:251], v240 offset:21504\n"
    "ds_read_b128 a[252:255], v240 offset:22016\n"
    "s_add_u32 m0, s54, 135168\n"
    "s_mov_b32 s59, 18432\n"
    "buffer_load_dwordx4 v251, s[44:47], s59 offen offset:0 nt lds\n"
    "ds_read_b128 v[96:99], v240 offset:2048\n"
    "ds_read_b128 v[100:103], v240 offset:2560\n"
    "ds_read_b128 v[104:107], v240 offset:6144\n"
    "ds_read_b128 v[108:111], v240 offset:6656\n"
    "s_add_u32 m0, s54, 136064\n"
    "s_mov_b32 s59, 18432\n"
    "buffer_load_dwordx4 v251, s[44:47], s59 offen offset:128 nt lds\n"
    "ds_read_b128 v[112:115], v240 offset:18432\n"
    "ds_read_b128 v[116:119], v240 offset:18944\n"
    "ds_read_b128 v[48:51], v240 offset:22528\n"
    "ds_read_b128 v[52:55], v240 offset:23040\n"
    "s_add_u32 m0, s54, 136960\n"
    "s_mov_b32 s59, 18432\n"
    "buffer_load_dwordx4 v251, s[44:47], s59 offen offset:256 nt lds\n"
    "ds_read_b128 v[56:59], v240 offset:3072\n"
    "ds_read_b128 v[60:63], v240 offset:3584\n"
    "ds_read_b128 v[64:67], v240 offset:7168\n"
    "ds_read_b128 v[68:71], v240 offset:7680\n"
    "s_add_u32 m0, s54, 137856\n"
    "s_mov_b32 s59, 18432\n"
    "buffer_load_dwordx4 v251, s[44:47], s59 offen offset:384 nt lds\n"
    "ds_read_b128 a[0:3], v240 offset:19456\n"
    "ds_read_b128 a[4:7], v240 offset:19968\n"
    "ds_read_b128 a[8:11], v240 offset:23552\n"
    "ds_read_b128 a[12:15], v240 offset:24064\n"
    "s_waitcnt lgkmcnt(0)\n"
    "v_mfma_f32_16x16x32_bf16 v[0:3], a[192:195], v[120:123], 0\n"
    "v_mfma_f32_16x16x32_bf16 v[0:3], a[196:199], v[124:127], v[0:3]\n"
    "v_mfma_f32_16x16x32_bf16 v[0:3], a[200:203], v[128:131], v[0:3]\n"
    "v_mfma_f32_16x16x32_bf16 v[0:3], a[204:207], v[132:135], v[0:3]\n"
    "v_mfma_f32_16x16x32_bf16 v[0:3], a[208:211], v[136:139], v[0:3]\n"
    "v_mfma_f32_16x16x32_bf16 v[0:3], a[212:215], v[140:143], v[0:3]\n"
    "v_mfma_f32_16x16x32_bf16 v[0:3], a[216:219], v[144:147], v[0:3]\n"
    "v_mfma_f32_16x16x32_bf16 v[0:3], a[220:223], v[148:151], v[0:3]\n"
    "v_mfma_f32_16x16x32_bf16 v[12:15], a[252:255], v[148:151], 0\n"
    "s_nop 0\n"
    "v_mfma_f32_16x16x32_bf16 v[12:15], a[248:251], v[144:147], v[12:15]\n"
    "s_nop 0\n"
    "v_mfma_f32_16x16x32_bf16 v[12:15], a[244:247], v[140:143], v[12:15]\n"
    "s_nop 0\n"
    "v_mfma_f32_16x16x32_bf16 v[12:15], a[240:243], v[136:139], v[12:15]\n"
    "s_nop 0\n"
    "v_mfma_f32_16x16x32_bf16 v[12:15], a[236:239], v[132:135], v[12:15]\n"
    "v_max3_f32 v232, v0, v1, v2\n"
    "v_max_f32_e32 v232, v232, v3\n"
    "v_mfma_f32_16x16x32_bf16 v[12:15], a[232:235], v[128:131], v[12:15]\n"
    "v_mov_b32_e32 v233, v232\n"
    "v_mfma_f32_16x16x32_bf16 v[12:15], a[228:231], v[124:127], v[12:15]\n"
    "v_mfma_f32_16x16x32_bf16 v[12:15], a[224:227], v[120:123], v[12:15]\n"
    "v_permlane16_swap_b32_e32 v233, v232\n"
    "v_mfma_f32_16x16x32_bf16 v[24:27], v[96:99], v[120:123], 0\n"
    "v_max_f32_e32 v232, v232, v233\n"
    "v_mov_b32_e32 v233, v232\n"
    "v_mfma_f32_16x16x32_bf16 v[24:27], v[100:103], v[124:127], v[24:27]\n"
    "v_mfma_f32_16x16x32_bf16 v[24:27], v[104:107], v[128:131], v[24:27]\n"
    "v_permlane32_swap_b32_e32 v233, v232\n"
    "v_mfma_f32_16x16x32_bf16 v[24:27], v[108:111], v[132:135], v[24:27]\n"
    "v_max_f32_e32 v232, v232, v233\n"
    "v_sub_f32_e32 v216, 0, v232\n"
    "v_mfma_f32_16x16x32_bf16 v[24:27], v[112:115], v[136:139], v[24:27]\n"
    "v_sub_f32_e32 v217, 0, v232\n"
    "v_sub_f32_e32 v218, 0, v232\n"
    "v_mfma_f32_16x16x32_bf16 v[24:27], v[116:119], v[140:143], v[24:27]\n"
    "v_sub_f32_e32 v219, 0, v232\n"
    "v_sub_f32_e32 v2, v2, v232\n"
    "v_mfma_f32_16x16x32_bf16 v[24:27], v[48:51], v[144:147], v[24:27]\n"
    "v_sub_f32_e32 v0, v0, v232\n"
    "v_sub_f32_e32 v1, v1, v232\n"
    "v_mfma_f32_16x16x32_bf16 v[24:27], v[52:55], v[148:151], v[24:27]\n"
    "v_exp_f32_e32 v72, v0\n"
    "v_mfma_f32_16x16x32_bf16 v[36:39], a[12:15], v[148:151], v[216:219]\n"
    "v_exp_f32_e32 v73, v1\n"
    "v_mfma_f32_16x16x32_bf16 v[36:39], a[8:11], v[144:147], v[36:39]\n"
    "v_exp_f32_e32 v74, v2\n"
    "v_mfma_f32_16x16x32_bf16 v[36:39], a[4:7], v[140:143], v[36:39]\n"
    "v_sub_f32_e32 v12, v12, v232\n"
    "v_sub_f32_e32 v14, v14, v232\n"
    "v_mfma_f32_16x16x32_bf16 v[36:39], a[0:3], v[136:139], v[36:39]\n"
    "v_exp_f32_e32 v84, v12\n"
    "v_mfma_f32_16x16x32_bf16 v[36:39], v[68:71], v[132:135], v[36:39]\n"
    "v_sub_f32_e32 v3, v3, v232\n"
    "v_sub_f32_e32 v15, v15, v232\n"
    "v_mfma_f32_16x16x32_bf16 v[36:39], v[64:67], v[128:131], v[36:39]\n"
    "v_sub_f32_e32 v24, v24, v232\n"
    "v_sub_f32_e32 v25, v25, v232\n"
    "v_mfma_f32_16x16x32_bf16 v[36:39], v[60:63], v[124:127], v[36:39]\n"
    "v_exp_f32_e32 v75, v3\n"
    "v_mfma_f32_16x16x32_bf16 v[36:39], v[56:59], v[120:123], v[36:39]\n"
    "v_sub_f32_e32 v27, v27, v232\n"
    "v_sub_f32_e32 v26, v26, v232\n"
    "v_sub_f32_e32 v13, v13, v232\n"
    "v_add_f32_e32 v234, v72, v73\n"
    "v_exp_f32_e32 v85, v13\n"
    "v_exp_f32_e32 v87, v15\n"
    "v_exp_f32_e32 v86, v14\n"
    "v_add_f32_e32 v234, v74, v234\n"
    "v_add_f32_e32 v234, v75, v234\n"
    "v_add_f32_e32 v234, v84, v234\n"
    "v_add_f32_e32 v234, v85, v234\n"
    "v_add_f32_e32 v234, v86, v234\n"
    "v_add_f32_e32 v234, v87, v234\n"
    "s_waitcnt vmcnt(8)\n"
    "ds_read_b128 v[152:155], v252 offset:32768\n"
    "ds_read_b128 v[156:159], v252 offset:33280\n"
    "ds_read_b128 v[160:163], v252 offset:33792\n"
    "ds_read_b128 v[164:167], v252 offset:34304\n"
    "ds_read_b128 v[168:171], v252 offset:34816\n"
    "ds_read_b128 v[172:175], v252 offset:35328\n"
    "ds_read_b128 v[176:179], v252 offset:35840\n"
    "ds_read_b128 v[180:183], v252 offset:36352\n"
    "s_waitcnt lgkmcnt(0)\n"
    "s_add_u32 m0, s54, 32768\n"
    "s_nop 0\n"
    "buffer_load_dwordx4 v245, s[36:39], s52 offen offset:0 lds\n"
    "v_lshlrev_b32_e32 v228, 16, v152\n"
    "v_and_b32_e32 v229, 0xffff0000, v152\n"
    "v_lshlrev_b32_e32 v230, 16, v153\n"
    "v_and_b32_e32 v231, 0xffff0000, v153\n"
    "v_pk_mul_f32 v[228:229], v[228:229], s[60:61]\n"
    "v_pk_mul_f32 v[230:231], v[230:231], s[60:61]\n"
    "v_cvt_pk_bf16_f32 v152, v228, v229\n"
    "v_cvt_pk_bf16_f32 v153, v230, v231\n"
    "v_lshlrev_b32_e32 v228, 16, v154\n"
    "v_and_b32_e32 v229, 0xffff0000, v154\n"
    "v_lshlrev_b32_e32 v230, 16, v155\n"
    "v_and_b32_e32 v231, 0xffff0000, v155\n"
    "v_pk_mul_f32 v[228:229], v[228:229], s[60:61]\n"
    "v_pk_mul_f32 v[230:231], v[230:231], s[60:61]\n"
    "v_cvt_pk_bf16_f32 v154, v228, v229\n"
    "v_cvt_pk_bf16_f32 v155, v230, v231\n"
    "buffer_load_dwordx4 v245, s[36:39], s52 offen offset:1024 lds\n"
    "v_lshlrev_b32_e32 v228, 16, v156\n"
    "v_and_b32_e32 v229, 0xffff0000, v156\n"
    "v_lshlrev_b32_e32 v230, 16, v157\n"
    "v_and_b32_e32 v231, 0xffff0000, v157\n"
    "v_pk_mul_f32 v[228:229], v[228:229], s[60:61]\n"
    "v_pk_mul_f32 v[230:231], v[230:231], s[60:61]\n"
    "v_cvt_pk_bf16_f32 v156, v228, v229\n"
    "v_cvt_pk_bf16_f32 v157, v230, v231\n"
    "v_lshlrev_b32_e32 v228, 16, v158\n"
    "v_and_b32_e32 v229, 0xffff0000, v158\n"
    "v_lshlrev_b32_e32 v230, 16, v159\n"
    "v_and_b32_e32 v231, 0xffff0000, v159\n"
    "v_pk_mul_f32 v[228:229], v[228:229], s[60:61]\n"
    "v_pk_mul_f32 v[230:231], v[230:231], s[60:61]\n"
    "v_cvt_pk_bf16_f32 v158, v228, v229\n"
    "v_cvt_pk_bf16_f32 v159, v230, v231\n"
    "buffer_load_dwordx4 v245, s[36:39], s52 offen offset:2048 lds\n"
    "v_lshlrev_b32_e32 v228, 16, v160\n"
    "v_and_b32_e32 v229, 0xffff0000, v160\n"
    "v_lshlrev_b32_e32 v230, 16, v161\n"
    "v_and_b32_e32 v231, 0xffff0000, v161\n"
    "v_pk_mul_f32 v[228:229], v[228:229], s[60:61]\n"
    "v_pk_mul_f32 v[230:231], v[230:231], s[60:61]\n"
    "v_cvt_pk_bf16_f32 v160, v228, v229\n"
    "v_cvt_pk_bf16_f32 v161, v230, v231\n"
    "v_lshlrev_b32_e32 v228, 16, v162\n"
    "v_and_b32_e32 v229, 0xffff0000, v162\n"
    "v_lshlrev_b32_e32 v230, 16, v163\n"
    "v_and_b32_e32 v231, 0xffff0000, v163\n"
    "v_pk_mul_f32 v[228:229], v[228:229], s[60:61]\n"
    "v_pk_mul_f32 v[230:231], v[230:231], s[60:61]\n"
    "v_cvt_pk_bf16_f32 v162, v228, v229\n"
    "v_cvt_pk_bf16_f32 v163, v230, v231\n"
    "buffer_load_dwordx4 v245, s[36:39], s52 offen offset:3072 lds\n"
    "v_lshlrev_b32_e32 v228, 16, v164\n"
    "v_and_b32_e32 v229, 0xffff0000, v164\n"
    "v_lshlrev_b32_e32 v230, 16, v165\n"
    "v_and_b32_e32 v231, 0xffff0000, v165\n"
    "v_pk_mul_f32 v[228:229], v[228:229], s[60:61]\n"
    "v_pk_mul_f32 v[230:231], v[230:231], s[60:61]\n"
    "v_cvt_pk_bf16_f32 v164, v228, v229\n"
    "v_cvt_pk_bf16_f32 v165, v230, v231\n"
    "v_lshlrev_b32_e32 v228, 16, v166\n"
    "v_and_b32_e32 v229, 0xffff0000, v166\n"
    "v_lshlrev_b32_e32 v230, 16, v167\n"
    "v_and_b32_e32 v231, 0xffff0000, v167\n"
    "v_pk_mul_f32 v[228:229], v[228:229], s[60:61]\n"
    "v_pk_mul_f32 v[230:231], v[230:231], s[60:61]\n"
    "v_cvt_pk_bf16_f32 v166, v228, v229\n"
    "v_cvt_pk_bf16_f32 v167, v230, v231\n"
    "s_add_u32 m0, s55, 32768\n"
    "s_nop 0\n"
    "buffer_load_dwordx4 v245, s[36:39], s52 offen offset:128 lds\n"
    "v_lshlrev_b32_e32 v228, 16, v168\n"
    "v_and_b32_e32 v229, 0xffff0000, v168\n"
    "v_lshlrev_b32_e32 v230, 16, v169\n"
    "v_and_b32_e32 v231, 0xffff0000, v169\n"
    "v_pk_mul_f32 v[228:229], v[228:229], s[60:61]\n"
    "v_pk_mul_f32 v[230:231], v[230:231], s[60:61]\n"
    "v_cvt_pk_bf16_f32 v168, v228, v229\n"
    "v_cvt_pk_bf16_f32 v169, v230, v231\n"
    "v_lshlrev_b32_e32 v228, 16, v170\n"
    "v_and_b32_e32 v229, 0xffff0000, v170\n"
    "v_lshlrev_b32_e32 v230, 16, v171\n"
    "v_and_b32_e32 v231, 0xffff0000, v171\n"
    "v_pk_mul_f32 v[228:229], v[228:229], s[60:61]\n"
    "v_pk_mul_f32 v[230:231], v[230:231], s[60:61]\n"
    "v_cvt_pk_bf16_f32 v170, v228, v229\n"
    "v_cvt_pk_bf16_f32 v171, v230, v231\n"
    "buffer_load_dwordx4 v245, s[36:39], s52 offen offset:1152 lds\n"
    "v_lshlrev_b32_e32 v228, 16, v172\n"
    "v_and_b32_e32 v229, 0xffff0000, v172\n"
    "v_lshlrev_b32_e32 v230, 16, v173\n"
    "v_and_b32_e32 v231, 0xffff0000, v173\n"
    "v_pk_mul_f32 v[228:229], v[228:229], s[60:61]\n"
    "v_pk_mul_f32 v[230:231], v[230:231], s[60:61]\n"
    "v_cvt_pk_bf16_f32 v172, v228, v229\n"
    "v_cvt_pk_bf16_f32 v173, v230, v231\n"
    "v_lshlrev_b32_e32 v228, 16, v174\n"
    "v_and_b32_e32 v229, 0xffff0000, v174\n"
    "v_lshlrev_b32_e32 v230, 16, v175\n"
    "v_and_b32_e32 v231, 0xffff0000, v175\n"
    "v_pk_mul_f32 v[228:229], v[228:229], s[60:61]\n"
    "v_pk_mul_f32 v[230:231], v[230:231], s[60:61]\n"
    "v_cvt_pk_bf16_f32 v174, v228, v229\n"
    "v_cvt_pk_bf16_f32 v175, v230, v231\n"
    "buffer_load_dwordx4 v245, s[36:39], s52 offen offset:2176 lds\n"
    "v_lshlrev_b32_e32 v228, 16, v176\n"
    "v_and_b32_e32 v229, 0xffff0000, v176\n"
    "v_lshlrev_b32_e32 v230, 16, v177\n"
    "v_and_b32_e32 v231, 0xffff0000, v177\n"
    "v_pk_mul_f32 v[228:229], v[228:229], s[60:61]\n"
    "v_pk_mul_f32 v[230:231], v[230:231], s[60:61]\n"
    "v_cvt_pk_bf16_f32 v176, v228, v229\n"
    "v_cvt_pk_bf16_f32 v177, v230, v231\n"
    "v_lshlrev_b32_e32 v228, 16, v178\n"
    "v_and_b32_e32 v229, 0xffff0000, v178\n"
    "v_lshlrev_b32_e32 v230, 16, v179\n"
    "v_and_b32_e32 v231, 0xffff0000, v179\n"
    "v_pk_mul_f32 v[228:229], v[228:229], s[60:61]\n"
    "v_pk_mul_f32 v[230:231], v[230:231], s[60:61]\n"
    "v_cvt_pk_bf16_f32 v178, v228, v229\n"
    "v_cvt_pk_bf16_f32 v179, v230, v231\n"
    "buffer_load_dwordx4 v245, s[36:39], s52 offen offset:3200 lds\n"
    "s_add_u32 s52, s52, 32768\n"
    "v_lshlrev_b32_e32 v228, 16, v180\n"
    "v_and_b32_e32 v229, 0xffff0000, v180\n"
    "v_lshlrev_b32_e32 v230, 16, v181\n"
    "v_and_b32_e32 v231, 0xffff0000, v181\n"
    "v_pk_mul_f32 v[228:229], v[228:229], s[60:61]\n"
    "v_pk_mul_f32 v[230:231], v[230:231], s[60:61]\n"
    "v_cvt_pk_bf16_f32 v180, v228, v229\n"
    "v_cvt_pk_bf16_f32 v181, v230, v231\n"
    "v_lshlrev_b32_e32 v228, 16, v182\n"
    "v_and_b32_e32 v229, 0xffff0000, v182\n"
    "v_lshlrev_b32_e32 v230, 16, v183\n"
    "v_and_b32_e32 v231, 0xffff0000, v183\n"
    "v_pk_mul_f32 v[228:229], v[228:229], s[60:61]\n"
    "v_pk_mul_f32 v[230:231], v[230:231], s[60:61]\n"
    "v_cvt_pk_bf16_f32 v182, v228, v229\n"
    "v_cvt_pk_bf16_f32 v183, v230, v231\n"
    "s_nop 1\n"
    "v_mfma_f32_16x16x32_bf16 v[4:7], a[192:195], v[152:155], 0\n"
    "v_mfma_f32_16x16x32_bf16 v[4:7], a[196:199], v[156:159], v[4:7]\n"
    "v_mfma_f32_16x16x32_bf16 v[4:7], a[200:203], v[160:163], v[4:7]\n"
    "v_mfma_f32_16x16x32_bf16 v[4:7], a[204:207], v[164:167], v[4:7]\n"
    "v_mfma_f32_16x16x32_bf16 v[4:7], a[208:211], v[168:171], v[4:7]\n"
    "v_mfma_f32_16x16x32_bf16 v[4:7], a[212:215], v[172:175], v[4:7]\n"
    "v_mfma_f32_16x16x32_bf16 v[4:7], a[216:219], v[176:179], v[4:7]\n"
    "v_mfma_f32_16x16x32_bf16 v[4:7], a[220:223], v[180:183], v[4:7]\n"
    "v_mfma_f32_16x16x32_bf16 v[16:19], a[252:255], v[180:183], 0\n"
    "s_nop 0\n"
    "v_mfma_f32_16x16x32_bf16 v[16:19], a[248:251], v[176:179], v[16:19]\n"
    "s_nop 0\n"
    "v_mfma_f32_16x16x32_bf16 v[16:19], a[244:247], v[172:175], v[16:19]\n"
    "s_nop 0\n"
    "v_mfma_f32_16x16x32_bf16 v[16:19], a[240:243], v[168:171], v[16:19]\n"
    "s_nop 0\n"
    "v_mfma_f32_16x16x32_bf16 v[16:19], a[236:239], v[164:167], v[16:19]\n"
    "v_max3_f32 v232, v4, v5, v6\n"
    "v_max_f32_e32 v232, v232, v7\n"
    "v_mfma_f32_16x16x32_bf16 v[16:19], a[232:235], v[160:163], v[16:19]\n"
    "v_mov_b32_e32 v233, v232\n"
    "v_mfma_f32_16x16x32_bf16 v[16:19], a[228:231], v[156:159], v[16:19]\n"
    "v_mfma_f32_16x16x32_bf16 v[16:19], a[224:227], v[152:155], v[16:19]\n"
    "v_permlane16_swap_b32_e32 v233, v232\n"
    "v_mfma_f32_16x16x32_bf16 v[28:31], v[96:99], v[152:155], 0\n"
    "v_max_f32_e32 v232, v232, v233\n"
    "v_mov_b32_e32 v233, v232\n"
    "v_mfma_f32_16x16x32_bf16 v[28:31], v[100:103], v[156:159], v[28:31]\n"
    "v_mfma_f32_16x16x32_bf16 v[28:31], v[104:107], v[160:163], v[28:31]\n"
    "v_permlane32_swap_b32_e32 v233, v232\n"
    "v_mfma_f32_16x16x32_bf16 v[28:31], v[108:111], v[164:167], v[28:31]\n"
    "v_max_f32_e32 v232, v232, v233\n"
    "v_sub_f32_e32 v220, 0, v232\n"
    "v_mfma_f32_16x16x32_bf16 v[28:31], v[112:115], v[168:171], v[28:31]\n"
    "v_sub_f32_e32 v221, 0, v232\n"
    "v_sub_f32_e32 v222, 0, v232\n"
    "v_mfma_f32_16x16x32_bf16 v[28:31], v[116:119], v[172:175], v[28:31]\n"
    "v_sub_f32_e32 v223, 0, v232\n"
    "v_sub_f32_e32 v6, v6, v232\n"
    "v_mfma_f32_16x16x32_bf16 v[28:31], v[48:51], v[176:179], v[28:31]\n"
    "v_sub_f32_e32 v4, v4, v232\n"
    "v_sub_f32_e32 v5, v5, v232\n"
    "v_mfma_f32_16x16x32_bf16 v[28:31], v[52:55], v[180:183], v[28:31]\n"
    "v_exp_f32_e32 v76, v4\n"
    "v_mfma_f32_16x16x32_bf16 v[40:43], a[12:15], v[180:183], v[220:223]\n"
    "v_exp_f32_e32 v77, v5\n"
    "v_mfma_f32_16x16x32_bf16 v[40:43], a[8:11], v[176:179], v[40:43]\n"
    "v_exp_f32_e32 v78, v6\n"
    "v_mfma_f32_16x16x32_bf16 v[40:43], a[4:7], v[172:175], v[40:43]\n"
    "v_sub_f32_e32 v16, v16, v232\n"
    "v_sub_f32_e32 v18, v18, v232\n"
    "v_mfma_f32_16x16x32_bf16 v[40:43], a[0:3], v[168:171], v[40:43]\n"
    "v_exp_f32_e32 v88, v16\n"
    "v_mfma_f32_16x16x32_bf16 v[40:43], v[68:71], v[164:167], v[40:43]\n"
    "v_sub_f32_e32 v7, v7, v232\n"
    "v_sub_f32_e32 v19, v19, v232\n"
    "v_mfma_f32_16x16x32_bf16 v[40:43], v[64:67], v[160:163], v[40:43]\n"
    "v_sub_f32_e32 v28, v28, v232\n"
    "v_sub_f32_e32 v29, v29, v232\n"
    "v_mfma_f32_16x16x32_bf16 v[40:43], v[60:63], v[156:159], v[40:43]\n"
    "v_exp_f32_e32 v79, v7\n"
    "v_mfma_f32_16x16x32_bf16 v[40:43], v[56:59], v[152:155], v[40:43]\n"
    "v_sub_f32_e32 v31, v31, v232\n"
    "v_sub_f32_e32 v30, v30, v232\n"
    "v_sub_f32_e32 v17, v17, v232\n"
    "v_add_f32_e32 v235, v76, v77\n"
    "v_exp_f32_e32 v89, v17\n"
    "v_exp_f32_e32 v91, v19\n"
    "v_exp_f32_e32 v90, v18\n"
    "v_add_f32_e32 v235, v78, v235\n"
    "v_add_f32_e32 v235, v79, v235\n"
    "v_add_f32_e32 v235, v88, v235\n"
    "v_add_f32_e32 v235, v89, v235\n"
    "v_add_f32_e32 v235, v90, v235\n"
    "v_add_f32_e32 v235, v91, v235\n"
    "s_waitcnt vmcnt(8)\n"
    "ds_read_b128 v[184:187], v253 offset:0\n"
    "ds_read_b128 v[188:191], v253 offset:512\n"
    "ds_read_b128 v[192:195], v253 offset:1024\n"
    "ds_read_b128 v[196:199], v253 offset:1536\n"
    "ds_read_b128 v[200:203], v253 offset:2048\n"
    "ds_read_b128 v[204:207], v253 offset:2560\n"
    "ds_read_b128 v[208:211], v253 offset:3072\n"
    "ds_read_b128 v[212:215], v253 offset:3584\n"
    "s_waitcnt lgkmcnt(0)\n"
    "ds_write_b32 v247, v250\n"
    "s_add_u32 m0, s54, 65536\n"
    "s_nop 0\n"
    "buffer_load_dwordx4 v246, s[40:43], s53 offen offset:0 nt lds\n"
    "v_lshlrev_b32_e32 v228, 16, v184\n"
    "v_and_b32_e32 v229, 0xffff0000, v184\n"
    "v_lshlrev_b32_e32 v230, 16, v185\n"
    "v_and_b32_e32 v231, 0xffff0000, v185\n"
    "v_pk_mul_f32 v[228:229], v[228:229], s[60:61]\n"
    "v_pk_mul_f32 v[230:231], v[230:231], s[60:61]\n"
    "v_cvt_pk_bf16_f32 v184, v228, v229\n"
    "v_cvt_pk_bf16_f32 v185, v230, v231\n"
    "v_lshlrev_b32_e32 v228, 16, v186\n"
    "v_and_b32_e32 v229, 0xffff0000, v186\n"
    "v_lshlrev_b32_e32 v230, 16, v187\n"
    "v_and_b32_e32 v231, 0xffff0000, v187\n"
    "v_pk_mul_f32 v[228:229], v[228:229], s[60:61]\n"
    "v_pk_mul_f32 v[230:231], v[230:231], s[60:61]\n"
    "v_cvt_pk_bf16_f32 v186, v228, v229\n"
    "v_cvt_pk_bf16_f32 v187, v230, v231\n"
    "buffer_load_dwordx4 v246, s[40:43], s53 offen offset:1024 nt lds\n"
    "v_lshlrev_b32_e32 v228, 16, v188\n"
    "v_and_b32_e32 v229, 0xffff0000, v188\n"
    "v_lshlrev_b32_e32 v230, 16, v189\n"
    "v_and_b32_e32 v231, 0xffff0000, v189\n"
    "v_pk_mul_f32 v[228:229], v[228:229], s[60:61]\n"
    "v_pk_mul_f32 v[230:231], v[230:231], s[60:61]\n"
    "v_cvt_pk_bf16_f32 v188, v228, v229\n"
    "v_cvt_pk_bf16_f32 v189, v230, v231\n"
    "v_lshlrev_b32_e32 v228, 16, v190\n"
    "v_and_b32_e32 v229, 0xffff0000, v190\n"
    "v_lshlrev_b32_e32 v230, 16, v191\n"
    "v_and_b32_e32 v231, 0xffff0000, v191\n"
    "v_pk_mul_f32 v[228:229], v[228:229], s[60:61]\n"
    "v_pk_mul_f32 v[230:231], v[230:231], s[60:61]\n"
    "v_cvt_pk_bf16_f32 v190, v228, v229\n"
    "v_cvt_pk_bf16_f32 v191, v230, v231\n"
    "buffer_load_dwordx4 v246, s[40:43], s53 offen offset:2048 nt lds\n"
    "v_lshlrev_b32_e32 v228, 16, v192\n"
    "v_and_b32_e32 v229, 0xffff0000, v192\n"
    "v_lshlrev_b32_e32 v230, 16, v193\n"
    "v_and_b32_e32 v231, 0xffff0000, v193\n"
    "v_pk_mul_f32 v[228:229], v[228:229], s[60:61]\n"
    "v_pk_mul_f32 v[230:231], v[230:231], s[60:61]\n"
    "v_cvt_pk_bf16_f32 v192, v228, v229\n"
    "v_cvt_pk_bf16_f32 v193, v230, v231\n"
    "v_lshlrev_b32_e32 v228, 16, v194\n"
    "v_and_b32_e32 v229, 0xffff0000, v194\n"
    "v_lshlrev_b32_e32 v230, 16, v195\n"
    "v_and_b32_e32 v231, 0xffff0000, v195\n"
    "v_pk_mul_f32 v[228:229], v[228:229], s[60:61]\n"
    "v_pk_mul_f32 v[230:231], v[230:231], s[60:61]\n"
    "v_cvt_pk_bf16_f32 v194, v228, v229\n"
    "v_cvt_pk_bf16_f32 v195, v230, v231\n"
    "buffer_load_dwordx4 v246, s[40:43], s53 offen offset:3072 nt lds\n"
    "v_lshlrev_b32_e32 v228, 16, v196\n"
    "v_and_b32_e32 v229, 0xffff0000, v196\n"
    "v_lshlrev_b32_e32 v230, 16, v197\n"
    "v_and_b32_e32 v231, 0xffff0000, v197\n"
    "v_pk_mul_f32 v[228:229], v[228:229], s[60:61]\n"
    "v_pk_mul_f32 v[230:231], v[230:231], s[60:61]\n"
    "v_cvt_pk_bf16_f32 v196, v228, v229\n"
    "v_cvt_pk_bf16_f32 v197, v230, v231\n"
    "v_lshlrev_b32_e32 v228, 16, v198\n"
    "v_and_b32_e32 v229, 0xffff0000, v198\n"
    "v_lshlrev_b32_e32 v230, 16, v199\n"
    "v_and_b32_e32 v231, 0xffff0000, v199\n"
    "v_pk_mul_f32 v[228:229], v[228:229], s[60:61]\n"
    "v_pk_mul_f32 v[230:231], v[230:231], s[60:61]\n"
    "v_cvt_pk_bf16_f32 v198, v228, v229\n"
    "v_cvt_pk_bf16_f32 v199, v230, v231\n"
    "s_add_u32 m0, s55, 65536\n"
    "s_nop 0\n"
    "buffer_load_dwordx4 v246, s[40:43], s53 offen offset:128 nt lds\n"
    "v_lshlrev_b32_e32 v228, 16, v200\n"
    "v_and_b32_e32 v229, 0xffff0000, v200\n"
    "v_lshlrev_b32_e32 v230, 16, v201\n"
    "v_and_b32_e32 v231, 0xffff0000, v201\n"
    "v_pk_mul_f32 v[228:229], v[228:229], s[60:61]\n"
    "v_pk_mul_f32 v[230:231], v[230:231], s[60:61]\n"
    "v_cvt_pk_bf16_f32 v200, v228, v229\n"
    "v_cvt_pk_bf16_f32 v201, v230, v231\n"
    "v_lshlrev_b32_e32 v228, 16, v202\n"
    "v_and_b32_e32 v229, 0xffff0000, v202\n"
    "v_lshlrev_b32_e32 v230, 16, v203\n"
    "v_and_b32_e32 v231, 0xffff0000, v203\n"
    "v_pk_mul_f32 v[228:229], v[228:229], s[60:61]\n"
    "v_pk_mul_f32 v[230:231], v[230:231], s[60:61]\n"
    "v_cvt_pk_bf16_f32 v202, v228, v229\n"
    "v_cvt_pk_bf16_f32 v203, v230, v231\n"
    "buffer_load_dwordx4 v246, s[40:43], s53 offen offset:1152 nt lds\n"
    "v_lshlrev_b32_e32 v228, 16, v204\n"
    "v_and_b32_e32 v229, 0xffff0000, v204\n"
    "v_lshlrev_b32_e32 v230, 16, v205\n"
    "v_and_b32_e32 v231, 0xffff0000, v205\n"
    "v_pk_mul_f32 v[228:229], v[228:229], s[60:61]\n"
    "v_pk_mul_f32 v[230:231], v[230:231], s[60:61]\n"
    "v_cvt_pk_bf16_f32 v204, v228, v229\n"
    "v_cvt_pk_bf16_f32 v205, v230, v231\n"
    "v_lshlrev_b32_e32 v228, 16, v206\n"
    "v_and_b32_e32 v229, 0xffff0000, v206\n"
    "v_lshlrev_b32_e32 v230, 16, v207\n"
    "v_and_b32_e32 v231, 0xffff0000, v207\n"
    "v_pk_mul_f32 v[228:229], v[228:229], s[60:61]\n"
    "v_pk_mul_f32 v[230:231], v[230:231], s[60:61]\n"
    "v_cvt_pk_bf16_f32 v206, v228, v229\n"
    "v_cvt_pk_bf16_f32 v207, v230, v231\n"
    "buffer_load_dwordx4 v246, s[40:43], s53 offen offset:2176 nt lds\n"
    "v_lshlrev_b32_e32 v228, 16, v208\n"
    "v_and_b32_e32 v229, 0xffff0000, v208\n"
    "v_lshlrev_b32_e32 v230, 16, v209\n"
    "v_and_b32_e32 v231, 0xffff0000, v209\n"
    "v_pk_mul_f32 v[228:229], v[228:229], s[60:61]\n"
    "v_pk_mul_f32 v[230:231], v[230:231], s[60:61]\n"
    "v_cvt_pk_bf16_f32 v208, v228, v229\n"
    "v_cvt_pk_bf16_f32 v209, v230, v231\n"
    "v_lshlrev_b32_e32 v228, 16, v210\n"
    "v_and_b32_e32 v229, 0xffff0000, v210\n"
    "v_lshlrev_b32_e32 v230, 16, v211\n"
    "v_and_b32_e32 v231, 0xffff0000, v211\n"
    "v_pk_mul_f32 v[228:229], v[228:229], s[60:61]\n"
    "v_pk_mul_f32 v[230:231], v[230:231], s[60:61]\n"
    "v_cvt_pk_bf16_f32 v210, v228, v229\n"
    "v_cvt_pk_bf16_f32 v211, v230, v231\n"
    "buffer_load_dwordx4 v246, s[40:43], s53 offen offset:3200 nt lds\n"
    "s_add_u32 s53, s53, 32768\n"
    "v_lshlrev_b32_e32 v228, 16, v212\n"
    "v_and_b32_e32 v229, 0xffff0000, v212\n"
    "v_lshlrev_b32_e32 v230, 16, v213\n"
    "v_and_b32_e32 v231, 0xffff0000, v213\n"
    "v_pk_mul_f32 v[228:229], v[228:229], s[60:61]\n"
    "v_pk_mul_f32 v[230:231], v[230:231], s[60:61]\n"
    "v_cvt_pk_bf16_f32 v212, v228, v229\n"
    "v_cvt_pk_bf16_f32 v213, v230, v231\n"
    "v_lshlrev_b32_e32 v228, 16, v214\n"
    "v_and_b32_e32 v229, 0xffff0000, v214\n"
    "v_lshlrev_b32_e32 v230, 16, v215\n"
    "v_and_b32_e32 v231, 0xffff0000, v215\n"
    "v_pk_mul_f32 v[228:229], v[228:229], s[60:61]\n"
    "v_pk_mul_f32 v[230:231], v[230:231], s[60:61]\n"
    "v_cvt_pk_bf16_f32 v214, v228, v229\n"
    "v_cvt_pk_bf16_f32 v215, v230, v231\n"
    "s_nop 1\n"
    "v_mfma_f32_16x16x32_bf16 v[8:11], a[192:195], v[184:187], 0\n"
    "v_mfma_f32_16x16x32_bf16 v[8:11], a[196:199], v[188:191], v[8:11]\n"
    "v_mfma_f32_16x16x32_bf16 v[8:11], a[200:203], v[192:195], v[8:11]\n"
    "v_mfma_f32_16x16x32_bf16 v[8:11], a[204:207], v[196:199], v[8:11]\n"
    "v_mfma_f32_16x16x32_bf16 v[8:11], a[208:211], v[200:203], v[8:11]\n"
    "v_mfma_f32_16x16x32_bf16 v[8:11], a[212:215], v[204:207], v[8:11]\n"
    "v_mfma_f32_16x16x32_bf16 v[8:11], a[216:219], v[208:211], v[8:11]\n"
    "v_mfma_f32_16x16x32_bf16 v[8:11], a[220:223], v[212:215], v[8:11]\n"
    "v_mfma_f32_16x16x32_bf16 v[20:23], a[252:255], v[212:215], 0\n"
    "s_nop 0\n"
    "v_mfma_f32_16x16x32_bf16 v[20:23], a[248:251], v[208:211], v[20:23]\n"
    "s_nop 0\n"
    "v_mfma_f32_16x16x32_bf16 v[20:23], a[244:247], v[204:207], v[20:23]\n"
    "s_nop 0\n"
    "v_mfma_f32_16x16x32_bf16 v[20:23], a[240:243], v[200:203], v[20:23]\n"
    "s_nop 0\n"
    "v_mfma_f32_16x16x32_bf16 v[20:23], a[236:239], v[196:199], v[20:23]\n"
    "v_max3_f32 v232, v8, v9, v10\n"
    "v_max_f32_e32 v232, v232, v11\n"
    "v_mfma_f32_16x16x32_bf16 v[20:23], a[232:235], v[192:195], v[20:23]\n"
    "v_mov_b32_e32 v233, v232\n"
    "v_mfma_f32_16x16x32_bf16 v[20:23], a[228:231], v[188:191], v[20:23]\n"
    "v_mfma_f32_16x16x32_bf16 v[20:23], a[224:227], v[184:187], v[20:23]\n"
    "v_permlane16_swap_b32_e32 v233, v232\n"
    "v_mfma_f32_16x16x32_bf16 v[32:35], v[96:99], v[184:187], 0\n"
    "v_max_f32_e32 v232, v232, v233\n"
    "v_mov_b32_e32 v233, v232\n"
    "v_mfma_f32_16x16x32_bf16 v[32:35], v[100:103], v[188:191], v[32:35]\n"
    "v_mfma_f32_16x16x32_bf16 v[32:35], v[104:107], v[192:195], v[32:35]\n"
    "v_permlane32_swap_b32_e32 v233, v232\n"
    "v_mfma_f32_16x16x32_bf16 v[32:35], v[108:111], v[196:199], v[32:35]\n"
    "v_max_f32_e32 v232, v232, v233\n"
    "v_sub_f32_e32 v224, 0, v232\n"
    "v_mfma_f32_16x16x32_bf16 v[32:35], v[112:115], v[200:203], v[32:35]\n"
    "v_sub_f32_e32 v225, 0, v232\n"
    "v_sub_f32_e32 v226, 0, v232\n"
    "v_mfma_f32_16x16x32_bf16 v[32:35], v[116:119], v[204:207], v[32:35]\n"
    "v_sub_f32_e32 v227, 0, v232\n"
    "v_sub_f32_e32 v10, v10, v232\n"
    "v_mfma_f32_16x16x32_bf16 v[32:35], v[48:51], v[208:211], v[32:35]\n"
    "v_sub_f32_e32 v11, v11, v232\n"
    "v_sub_f32_e32 v8, v8, v232\n"
    "v_mfma_f32_16x16x32_bf16 v[32:35], v[52:55], v[212:215], v[32:35]\n"
    "v_exp_f32_e32 v80, v8\n"
    "v_mfma_f32_16x16x32_bf16 v[44:47], a[12:15], v[212:215], v[224:227]\n"
    "v_sub_f32_e32 v23, v23, v232\n"
    "v_sub_f32_e32 v9, v9, v232\n"
    "v_mfma_f32_16x16x32_bf16 v[44:47], a[8:11], v[208:211], v[44:47]\n"
    "v_exp_f32_e32 v81, v9\n"
    "v_mfma_f32_16x16x32_bf16 v[44:47], a[4:7], v[204:207], v[44:47]\n"
    "v_exp_f32_e32 v82, v10\n"
    "v_mfma_f32_16x16x32_bf16 v[44:47], a[0:3], v[200:203], v[44:47]\n"
    "v_exp_f32_e32 v83, v11\n"
    "v_mfma_f32_16x16x32_bf16 v[44:47], v[68:71], v[196:199], v[44:47]\n"
    "v_sub_f32_e32 v22, v22, v232\n"
    "v_sub_f32_e32 v20, v20, v232\n"
    "v_mfma_f32_16x16x32_bf16 v[44:47], v[64:67], v[192:195], v[44:47]\n"
    "v_exp_f32_e32 v92, v20\n"
    "v_mfma_f32_16x16x32_bf16 v[44:47], v[60:63], v[188:191], v[44:47]\n"
    "v_sub_f32_e32 v32, v32, v232\n"
    "v_sub_f32_e32 v34, v34, v232\n"
    "v_mfma_f32_16x16x32_bf16 v[44:47], v[56:59], v[184:187], v[44:47]\n"
    "v_sub_f32_e32 v21, v21, v232\n"
    "v_sub_f32_e32 v35, v35, v232\n"
    "v_exp_f32_e32 v93, v21\n"
    "v_sub_f32_e32 v33, v33, v232\n"
    "v_add_f32_e32 v236, v80, v81\n"
    "v_add_f32_e32 v236, v82, v236\n"
    "v_accvgpr_write_b32 a14, 0\n"
    "v_add_f32_e32 v236, v83, v236\n"
    "v_accvgpr_write_b32 a2, 0\n"
    "v_accvgpr_write_b32 a0, 0\n"
    "v_accvgpr_write_b32 a6, 0\n"
    "v_add_f32_e32 v236, v92, v236\n"
    "v_accvgpr_write_b32 a10, 0\n"
    "v_accvgpr_write_b32 a9, 0\n"
    "v_add_f32_e32 v236, v93, v236\n"
    "v_accvgpr_write_b32 a8, 0\n"
    "v_accvgpr_write_b32 a11, 0\n"
    "v_accvgpr_write_b32 a1, 0\n"
    "v_accvgpr_write_b32 a3, 0\n"
    "v_accvgpr_write_b32 a5, 0\n"
    "v_accvgpr_write_b32 a15, 0\n"
    "v_accvgpr_write_b32 a13, 0\n"
    "v_accvgpr_write_b32 a4, 0\n"
    "v_accvgpr_write_b32 a7, 0\n"
    "v_accvgpr_write_b32 a12, 0\n"
    "s_waitcnt vmcnt(8) lgkmcnt(0)\n"
    "s_barrier\n"
    "ds_read_b128 a[192:195], v240 offset:32768\n"
    "ds_read_b128 a[196:199], v240 offset:33280\n"
    "ds_read_b128 a[200:203], v240 offset:36864\n"
    "ds_read_b128 a[204:207], v240 offset:37376\n"
    "ds_read_b128 a[208:211], v240 offset:49152\n"
    "ds_read_b128 a[212:215], v240 offset:49664\n"
    "ds_read_b128 a[216:219], v240 offset:53248\n"
    "ds_read_b128 a[220:223], v240 offset:53760\n"
    "s_add_u32 m0, s54, 0\n"
    "s_nop 0\n"
    "buffer_load_dwordx4 v245, s[36:39], s52 offen offset:0 lds\n"
    "buffer_load_dwordx4 v245, s[36:39], s52 offen offset:1024 lds\n"
    "buffer_load_dwordx4 v245, s[36:39], s52 offen offset:2048 lds\n"
    "buffer_load_dwordx4 v245, s[36:39], s52 offen offset:3072 lds\n"
    "s_add_u32 m0, s55, 0\n"
    "s_nop 0\n"
    "buffer_load_dwordx4 v245, s[36:39], s52 offen offset:128 lds\n"
    "buffer_load_dwordx4 v245, s[36:39], s52 offen offset:1152 lds\n"
    "buffer_load_dwordx4 v245, s[36:39], s52 offen offset:2176 lds\n"
    "buffer_load_dwordx4 v245, s[36:39], s52 offen offset:3200 lds\n"
    "s_add_u32 s52, s52, 32768\n"
    "s_waitcnt lgkmcnt(0)\n"
    "s_branch LOOP_%=\n"
    ".p2align 6\n"
    "LOOP_%=:\n"
    "v_mfma_f32_16x16x32_bf16 v[48:51], a[192:195], v[120:123], v[216:219]\n"
    "v_exp_f32_e32 v228, v22\n"
    "v_mfma_f32_16x16x32_bf16 v[48:51], a[196:199], v[124:127], v[48:51]\n"
    "v_exp_f32_e32 v229, v23\n"
    "v_mfma_f32_16x16x32_bf16 v[48:51], a[200:203], v[128:131], v[48:51]\n"
    "s_add_u32 m0, s54, 98304\n"
    "v_add_f32_e32 v236, v228, v236\n"
    "v_mfma_f32_16x16x32_bf16 v[48:51], a[204:207], v[132:135], v[48:51]\n"
    "s_waitcnt lgkmcnt(0)\n"
    "v_cvt_pk_bf16_f32 v96, v72, v73\n"
    "v_mfma_f32_16x16x32_bf16 v[48:51], a[208:211], v[136:139], v[48:51]\n"
    "buffer_load_dwordx4 v246, s[40:43], s53 offen offset:0 nt lds\n"
    "v_mfma_f32_16x16x32_bf16 v[48:51], a[212:215], v[140:143], v[48:51]\n"
    "ds_read_b128 a[224:227], v240 offset:33792\n"
    "v_add_f32_e32 v236, v229, v236\n"
    "v_mfma_f32_16x16x32_bf16 v[48:51], a[216:219], v[144:147], v[48:51]\n"
    "ds_read_b128 a[228:231], v240 offset:34304\n"
    "v_cvt_pk_bf16_f32 v115, v228, v229\n"
    "v_mfma_f32_16x16x32_bf16 v[48:51], a[220:223], v[148:151], v[48:51]\n"
    "ds_read_b128 a[232:235], v240 offset:37888\n"
    "v_cvt_pk_bf16_f32 v97, v74, v75\n"
    "v_mfma_f32_16x16x32_bf16 v[52:55], a[220:223], v[180:183], v[220:223]\n"
    "ds_read_b128 a[236:239], v240 offset:38400\n"
    "v_cvt_pk_bf16_f32 v104, v76, v77\n"
    "v_mfma_f32_16x16x32_bf16 v[52:55], a[216:219], v[176:179], v[52:55]\n"
    "v_exp_f32_e32 v230, v24\n"
    "v_mfma_f32_16x16x32_bf16 v[52:55], a[212:215], v[172:175], v[52:55]\n"
    "ds_read_b128 a[240:243], v240 offset:50176\n"
    "v_add_f32_e32 v234, v230, v234\n"
    "v_mfma_f32_16x16x32_bf16 v[52:55], a[208:211], v[168:171], v[52:55]\n"
    "buffer_load_dwordx4 v246, s[40:43], s53 offen offset:1024 nt lds\n"
    "v_mfma_f32_16x16x32_bf16 v[52:55], a[204:207], v[164:167], v[52:55]\n"
    "ds_read_b128 a[244:247], v240 offset:50688\n"
    "v_cvt_pk_bf16_f32 v112, v80, v81\n"
    "v_mfma_f32_16x16x32_bf16 v[52:55], a[200:203], v[160:163], v[52:55]\n"
    "ds_read_b128 a[248:251], v240 offset:54272\n"
    "v_cvt_pk_bf16_f32 v105, v78, v79\n"
    "v_mfma_f32_16x16x32_bf16 v[52:55], a[196:199], v[156:159], v[52:55]\n"
    "ds_read_b128 a[252:255], v240 offset:54784\n"
    "v_cvt_pk_bf16_f32 v113, v82, v83\n"
    "v_mfma_f32_16x16x32_bf16 v[52:55], a[192:195], v[152:155], v[52:55]\n"
    "v_exp_f32_e32 v231, v25\n"
    "v_mfma_f32_16x16x32_bf16 v[56:59], a[192:195], v[184:187], v[224:227]\n"
    "v_exp_f32_e32 v232, v26\n"
    "v_mfma_f32_16x16x32_bf16 v[56:59], a[196:199], v[188:191], v[56:59]\n"
    "buffer_load_dwordx4 v246, s[40:43], s53 offen offset:2048 nt lds\n"
    "v_mfma_f32_16x16x32_bf16 v[56:59], a[200:203], v[192:195], v[56:59]\n"
    "v_exp_f32_e32 v233, v27\n"
    "v_mfma_f32_16x16x32_bf16 v[56:59], a[204:207], v[196:199], v[56:59]\n"
    "v_add_f32_e32 v234, v231, v234\n"
    "v_cvt_pk_bf16_f32 v100, v230, v231\n"
    "v_mfma_f32_16x16x32_bf16 v[56:59], a[208:211], v[200:203], v[56:59]\n"
    "v_exp_f32_e32 v228, v28\n"
    "v_mfma_f32_16x16x32_bf16 v[56:59], a[212:215], v[204:207], v[56:59]\n"
    "v_exp_f32_e32 v229, v29\n"
    "v_mfma_f32_16x16x32_bf16 v[56:59], a[216:219], v[208:211], v[56:59]\n"
    "v_add_f32_e32 v234, v232, v234\n"
    "v_add_f32_e32 v234, v233, v234\n"
    "v_mfma_f32_16x16x32_bf16 v[56:59], a[220:223], v[212:215], v[56:59]\n"
    "s_waitcnt lgkmcnt(0)\n"
    "v_cvt_pk_bf16_f32 v101, v232, v233\n"
    "v_mfma_f32_16x16x32_bf16 v[68:71], a[252:255], v[212:215], v[224:227]\n"
    "buffer_load_dwordx4 v246, s[40:43], s53 offen offset:3072 nt lds\n"
    "v_mfma_f32_16x16x32_bf16 v[68:71], a[248:251], v[208:211], v[68:71]\n"
    "ds_read_b128 a[192:195], v240 offset:34816\n"
    "v_add_f32_e32 v235, v228, v235\n"
    "v_mfma_f32_16x16x32_bf16 v[68:71], a[244:247], v[204:207], v[68:71]\n"
    "ds_read_b128 a[196:199], v240 offset:35328\n"
    "v_add_f32_e32 v235, v229, v235\n"
    "v_mfma_f32_16x16x32_bf16 v[68:71], a[240:243], v[200:203], v[68:71]\n"
    "ds_read_b128 a[200:203], v240 offset:38912\n"
    "v_cvt_pk_bf16_f32 v108, v228, v229\n"
    "v_mfma_f32_16x16x32_bf16 v[68:71], a[236:239], v[196:199], v[68:71]\n"
    "v_exp_f32_e32 v232, v32\n"
    "v_mfma_f32_16x16x32_bf16 v[68:71], a[232:235], v[192:195], v[68:71]\n"
    "ds_read_b128 a[204:207], v240 offset:39424\n"
    "v_add_f32_e32 v236, v232, v236\n"
    "v_mfma_f32_16x16x32_bf16 v[68:71], a[228:231], v[188:191], v[68:71]\n"
    "s_add_u32 m0, s55, 98304\n"
    "ds_read_b128 a[208:211], v240 offset:51200\n"
    "v_mfma_f32_16x16x32_bf16 v[68:71], a[224:227], v[184:187], v[68:71]\n"
    "buffer_load_dwordx4 v246, s[40:43], s53 offen offset:128 nt lds\n"
    "v_mfma_f32_16x16x32_bf16 v[64:67], a[224:227], v[152:155], v[220:223]\n"
    "v_exp_f32_e32 v230, v30\n"
    "v_mfma_f32_16x16x32_bf16 v[64:67], a[228:231], v[156:159], v[64:67]\n"
    "v_exp_f32_e32 v231, v31\n"
    "v_mfma_f32_16x16x32_bf16 v[64:67], a[232:235], v[160:163], v[64:67]\n"
    "ds_read_b128 a[212:215], v240 offset:51712\n"
    "v_add_f32_e32 v235, v230, v235\n"
    "v_mfma_f32_16x16x32_bf16 v[64:67], a[236:239], v[164:167], v[64:67]\n"
    "v_exp_f32_e32 v233, v33\n"
    "v_mfma_f32_16x16x32_bf16 v[64:67], a[240:243], v[168:171], v[64:67]\n"
    "ds_read_b128 a[216:219], v240 offset:55296\n"
    "v_add_f32_e32 v235, v231, v235\n"
    "v_mfma_f32_16x16x32_bf16 v[64:67], a[244:247], v[172:175], v[64:67]\n"
    "ds_read_b128 a[220:223], v240 offset:55808\n"
    "v_cvt_pk_bf16_f32 v109, v230, v231\n"
    "v_mfma_f32_16x16x32_bf16 v[64:67], a[248:251], v[176:179], v[64:67]\n"
    "buffer_load_dwordx4 v246, s[40:43], s53 offen offset:1152 nt lds\n"
    "v_mfma_f32_16x16x32_bf16 v[64:67], a[252:255], v[180:183], v[64:67]\n"
    "v_exp_f32_e32 v228, v34\n"
    "v_mfma_f32_16x16x32_bf16 v[60:63], a[252:255], v[148:151], v[216:219]\n"
    "v_exp_f32_e32 v229, v35\n"
    "v_mfma_f32_16x16x32_bf16 v[60:63], a[248:251], v[144:147], v[60:63]\n"
    "v_exp_f32_e32 v230, v36\n"
    "v_mfma_f32_16x16x32_bf16 v[60:63], a[244:247], v[140:143], v[60:63]\n"
    "v_cvt_pk_bf16_f32 v116, v232, v233\n"
    "v_add_f32_e32 v236, v233, v236\n"
    "v_mfma_f32_16x16x32_bf16 v[60:63], a[240:243], v[136:139], v[60:63]\n"
    "buffer_load_dwordx4 v246, s[40:43], s53 offen offset:2176 nt lds\n"
    "v_mfma_f32_16x16x32_bf16 v[60:63], a[236:239], v[132:135], v[60:63]\n"
    "v_exp_f32_e32 v231, v37\n"
    "v_mfma_f32_16x16x32_bf16 v[60:63], a[232:235], v[128:131], v[60:63]\n"
    "v_exp_f32_e32 v232, v38\n"
    "v_mfma_f32_16x16x32_bf16 v[60:63], a[228:231], v[124:127], v[60:63]\n"
    "v_cvt_pk_bf16_f32 v98, v84, v85\n"
    "v_add_f32_e32 v236, v228, v236\n"
    "v_mfma_f32_16x16x32_bf16 v[60:63], a[224:227], v[120:123], v[60:63]\n"
    "s_waitcnt lgkmcnt(0)\n"
    "v_add_f32_e32 v236, v229, v236\n"
    "v_mfma_f32_16x16x32_bf16 v[72:75], a[192:195], v[120:123], v[216:219]\n"
    "ds_read_b128 a[224:227], v240 offset:35840\n"
    "v_cvt_pk_bf16_f32 v117, v228, v229\n"
    "v_mfma_f32_16x16x32_bf16 v[72:75], a[196:199], v[124:127], v[72:75]\n"
    "ds_read_b128 a[228:231], v240 offset:36352\n"
    "v_cvt_pk_bf16_f32 v99, v86, v87\n"
    "v_mfma_f32_16x16x32_bf16 v[72:75], a[200:203], v[128:131], v[72:75]\n"
    "ds_read_b128 a[232:235], v240 offset:39936\n"
    "v_cvt_pk_bf16_f32 v102, v230, v231\n"
    "v_mfma_f32_16x16x32_bf16 v[72:75], a[204:207], v[132:135], v[72:75]\n"
    "ds_read_b128 a[236:239], v240 offset:40448\n"
    "v_add_f32_e32 v234, v230, v234\n"
    "v_mfma_f32_16x16x32_bf16 v[72:75], a[208:211], v[136:139], v[72:75]\n"
    "ds_read_b128 a[240:243], v240 offset:52224\n"
    "v_add_f32_e32 v234, v231, v234\n"
    "v_mfma_f32_16x16x32_bf16 v[72:75], a[212:215], v[140:143], v[72:75]\n"
    "buffer_load_dwordx4 v246, s[40:43], s53 offen offset:3200 nt lds\n"
    "v_mfma_f32_16x16x32_bf16 v[72:75], a[216:219], v[144:147], v[72:75]\n"
    "ds_read_b128 a[244:247], v240 offset:52736\n"
    "v_add_f32_e32 v234, v232, v234\n"
    "v_mfma_f32_16x16x32_bf16 v[72:75], a[220:223], v[148:151], v[72:75]\n"
    "ds_read_b128 a[248:251], v240 offset:56320\n"
    "v_cvt_pk_bf16_f32 v106, v88, v89\n"
    "v_mfma_f32_16x16x32_bf16 v[76:79], a[220:223], v[180:183], v[220:223]\n"
    "ds_read_b128 a[252:255], v240 offset:56832\n"
    "v_cvt_pk_bf16_f32 v107, v90, v91\n"
    "v_mfma_f32_16x16x32_bf16 v[76:79], a[216:219], v[176:179], v[76:79]\n"
    "v_exp_f32_e32 v233, v39\n"
    "v_mfma_f32_16x16x32_bf16 v[76:79], a[212:215], v[172:175], v[76:79]\n"
    "v_exp_f32_e32 v230, v42\n"
    "v_mfma_f32_16x16x32_bf16 v[76:79], a[208:211], v[168:171], v[76:79]\n"
    "v_exp_f32_e32 v228, v40\n"
    "v_mfma_f32_16x16x32_bf16 v[76:79], a[204:207], v[164:167], v[76:79]\n"
    "v_exp_f32_e32 v229, v41\n"
    "v_mfma_f32_16x16x32_bf16 v[76:79], a[200:203], v[160:163], v[76:79]\n"
    "v_exp_f32_e32 v231, v43\n"
    "v_mfma_f32_16x16x32_bf16 v[76:79], a[196:199], v[156:159], v[76:79]\n"
    "v_add_f32_e32 v234, v233, v234\n"
    "v_add_f32_e32 v235, v228, v235\n"
    "v_mfma_f32_16x16x32_bf16 v[76:79], a[192:195], v[152:155], v[76:79]\n"
    "v_cvt_pk_bf16_f32 v103, v232, v233\n"
    "v_add_f32_e32 v235, v229, v235\n"
    "v_mfma_f32_16x16x32_bf16 v[80:83], a[192:195], v[184:187], v[224:227]\n"
    "v_exp_f32_e32 v232, v44\n"
    "v_mfma_f32_16x16x32_bf16 v[80:83], a[196:199], v[188:191], v[80:83]\n"
    "s_add_u32 s53, s53, 32768\n"
    "v_add_f32_e32 v235, v230, v235\n"
    "v_mfma_f32_16x16x32_bf16 v[80:83], a[200:203], v[192:195], v[80:83]\n"
    "v_exp_f32_e32 v233, v45\n"
    "v_mfma_f32_16x16x32_bf16 v[80:83], a[204:207], v[196:199], v[80:83]\n"
    "v_cvt_pk_bf16_f32 v110, v228, v229\n"
    "v_add_f32_e32 v235, v231, v235\n"
    "v_mfma_f32_16x16x32_bf16 v[80:83], a[208:211], v[200:203], v[80:83]\n"
    "v_cvt_pk_bf16_f32 v114, v92, v93\n"
    "v_cvt_pk_bf16_f32 v111, v230, v231\n"
    "v_mfma_f32_16x16x32_bf16 v[80:83], a[212:215], v[204:207], v[80:83]\n"
    "v_exp_f32_e32 v228, v46\n"
    "v_mfma_f32_16x16x32_bf16 v[80:83], a[216:219], v[208:211], v[80:83]\n"
    "v_exp_f32_e32 v229, v47\n"
    "v_mfma_f32_16x16x32_bf16 v[80:83], a[220:223], v[212:215], v[80:83]\n"
    "s_waitcnt vmcnt(16) lgkmcnt(0)\n"
    "s_barrier\n"
    "v_mfma_f32_16x16x32_bf16 v[92:95], a[252:255], v[212:215], v[224:227]\n"
    "ds_read_b64_tr_b16 a[192:193], v241 offset:0\n"
    "v_add_f32_e32 v236, v232, v236\n"
    "v_mfma_f32_16x16x32_bf16 v[92:95], a[248:251], v[208:211], v[92:95]\n"
    "ds_read_b64_tr_b16 a[194:195], v241 offset:1024\n"
    "v_cvt_pk_bf16_f32 v118, v232, v233\n"
    "v_mfma_f32_16x16x32_bf16 v[92:95], a[244:247], v[204:207], v[92:95]\n"
    "ds_read_b64_tr_b16 a[196:197], v241 offset:2048\n"
    "v_add_f32_e32 v236, v233, v236\n"
    "v_mfma_f32_16x16x32_bf16 v[92:95], a[240:243], v[200:203], v[92:95]\n"
    "ds_read_b64_tr_b16 a[198:199], v241 offset:3072\n"
    "v_add_f32_e32 v236, v228, v236\n"
    "v_mfma_f32_16x16x32_bf16 v[92:95], a[236:239], v[196:199], v[92:95]\n"
    "ds_read_b64_tr_b16 a[200:201], v242 offset:0\n"
    "v_cvt_pk_bf16_f32 v119, v228, v229\n"
    "v_mfma_f32_16x16x32_bf16 v[92:95], a[232:235], v[192:195], v[92:95]\n"
    "ds_read_b64_tr_b16 a[202:203], v242 offset:1024\n"
    "v_add_f32_e32 v236, v229, v236\n"
    "v_mfma_f32_16x16x32_bf16 v[92:95], a[228:231], v[188:191], v[92:95]\n"
    "ds_read_b64_tr_b16 a[204:205], v242 offset:2048\n"
    "v_max3_f32 v251, v234, v235, v236\n"
    "v_mfma_f32_16x16x32_bf16 v[92:95], a[224:227], v[184:187], v[92:95]\n"
    "ds_read_b64_tr_b16 a[206:207], v242 offset:3072\n"
    "s_sub_u32 s56, s56, 1\n"
    "v_mfma_f32_16x16x32_bf16 v[88:91], a[224:227], v[152:155], v[220:223]\n"
    "ds_read_b64_tr_b16 a[208:209], v243 offset:0\n"
    "s_cmp_eq_u32 s56, 0\n"
    "v_mfma_f32_16x16x32_bf16 v[88:91], a[228:231], v[156:159], v[88:91]\n"
    "ds_read_b64_tr_b16 a[210:211], v243 offset:1024\n"
    "v_cmp_lt_f32_e32 vcc, s57, v251\n"
    "v_mfma_f32_16x16x32_bf16 v[88:91], a[232:235], v[160:163], v[88:91]\n"
    "ds_read_b64_tr_b16 a[212:213], v243 offset:2048\n"
    "v_mfma_f32_16x16x32_bf16 v[88:91], a[236:239], v[164:167], v[88:91]\n"
    "ds_read_b64_tr_b16 a[214:215], v243 offset:3072\n"
    "v_mfma_f32_16x16x32_bf16 v[88:91], a[240:243], v[168:171], v[88:91]\n"
    "ds_read_b64_tr_b16 a[216:217], v244 offset:0\n"
    "v_mfma_f32_16x16x32_bf16 v[88:91], a[244:247], v[172:175], v[88:91]\n"
    "ds_read_b64_tr_b16 a[218:219], v244 offset:1024\n"
    "v_mfma_f32_16x16x32_bf16 v[88:91], a[248:251], v[176:179], v[88:91]\n"
    "ds_read_b64_tr_b16 a[220:221], v244 offset:2048\n"
    "v_mfma_f32_16x16x32_bf16 v[88:91], a[252:255], v[180:183], v[88:91]\n"
    "ds_read_b64_tr_b16 a[222:223], v244 offset:3072\n"
    "v_mfma_f32_16x16x32_bf16 v[84:87], a[252:255], v[148:151], v[216:219]\n"
    "v_mfma_f32_16x16x32_bf16 v[84:87], a[248:251], v[144:147], v[84:87]\n"
    "v_mfma_f32_16x16x32_bf16 v[84:87], a[244:247], v[140:143], v[84:87]\n"
    "v_mfma_f32_16x16x32_bf16 v[84:87], a[240:243], v[136:139], v[84:87]\n"
    "v_mfma_f32_16x16x32_bf16 v[84:87], a[236:239], v[132:135], v[84:87]\n"
    "v_mfma_f32_16x16x32_bf16 v[84:87], a[232:235], v[128:131], v[84:87]\n"
    "v_mfma_f32_16x16x32_bf16 v[84:87], a[228:231], v[124:127], v[84:87]\n"
    "s_waitcnt lgkmcnt(12)\n"
    "v_mfma_f32_16x16x32_bf16 v[84:87], a[224:227], v[120:123], v[84:87]\n"
    "s_cbranch_vccnz RARE1_%=\n"
    "RET1_%=:\n"
    "s_cbranch_scc1 TAIL_%=\n"
    "v_mfma_f32_16x16x32_bf16 a[0:3], v[96:99], a[192:195], a[0:3]\n"
    "s_add_u32 m0, s54, 32768\n"
    "ds_read_b64_tr_b16 a[224:225], v241 offset:4096\n"
    "v_mfma_f32_16x16x32_bf16 a[0:3], v[100:103], a[196:199], a[0:3]\n"
    "ds_read_b64_tr_b16 a[226:227], v241 offset:5120\n"
    "v_add_f32_e32 v238, v235, v238\n"
    "v_mfma_f32_16x16x32_bf16 a[4:7], v[108:111], a[196:199], a[4:7]\n"
    "ds_read_b64_tr_b16 a[228:229], v241 offset:6144\n"
    "v_add_f32_e32 v237, v234, v237\n"
    "v_mfma_f32_16x16x32_bf16 a[4:7], v[104:107], a[192:195], a[4:7]\n"
    "buffer_load_dwordx4 v245, s[36:39], s52 offen offset:0 lds\n"
    "v_mfma_f32_16x16x32_bf16 a[8:11], v[112:115], a[192:195], a[8:11]\n"
    "ds_read_b64_tr_b16 a[230:231], v241 offset:7168\n"
    "v_add_f32_e32 v239, v236, v239\n"
    "v_mfma_f32_16x16x32_bf16 a[8:11], v[116:119], a[196:199], a[8:11]\n"
    "s_waitcnt lgkmcnt(12)\n"
    "ds_read_b64_tr_b16 a[232:233], v242 offset:4096\n"
    "v_mfma_f32_16x16x32_bf16 a[20:23], v[116:119], a[204:207], a[20:23]\n"
    "ds_read_b64_tr_b16 a[234:235], v242 offset:5120\n"
    "v_mfma_f32_16x16x32_bf16 a[20:23], v[112:115], a[200:203], a[20:23]\n"
    "ds_read_b64_tr_b16 a[236:237], v242 offset:6144\n"
    "v_mfma_f32_16x16x32_bf16 a[16:19], v[104:107], a[200:203], a[16:19]\n"
    "ds_read_b64_tr_b16 a[238:239], v242 offset:7168\n"
    "v_mfma_f32_16x16x32_bf16 a[16:19], v[108:111], a[204:207], a[16:19]\n"
    "ds_read_b64_tr_b16 a[240:241], v243 offset:4096\n"
    "v_mfma_f32_16x16x32_bf16 a[12:15], v[100:103], a[204:207], a[12:15]\n"
    "buffer_load_dwordx4 v245, s[36:39], s52 offen offset:1024 lds\n"
    "v_mfma_f32_16x16x32_bf16 a[12:15], v[96:99], a[200:203], a[12:15]\n"
    "s_waitcnt lgkmcnt(9)\n"
    "ds_read_b64_tr_b16 a[242:243], v243 offset:5120\n"
    "v_mfma_f32_16x16x32_bf16 a[24:27], v[96:99], a[208:211], a[24:27]\n"
    "ds_read_b64_tr_b16 a[244:245], v243 offset:6144\n"
    "v_mfma_f32_16x16x32_bf16 a[24:27], v[100:103], a[212:215], a[24:27]\n"
    "ds_read_b64_tr_b16 a[246:247], v243 offset:7168\n"
    "v_mfma_f32_16x16x32_bf16 a[28:31], v[108:111], a[212:215], a[28:31]\n"
    "ds_read_b64_tr_b16 a[248:249], v244 offset:4096\n"
    "v_mfma_f32_16x16x32_bf16 a[28:31], v[104:107], a[208:211], a[28:31]\n"
    "ds_read_b64_tr_b16 a[250:251], v244 offset:5120\n"
    "v_mfma_f32_16x16x32_bf16 a[32:35], v[112:115], a[208:211], a[32:35]\n"
    "ds_read_b64_tr_b16 a[252:253], v244 offset:6144\n"
    "v_mfma_f32_16x16x32_bf16 a[32:35], v[116:119], a[212:215], a[32:35]\n"
    "buffer_load_dwordx4 v245, s[36:39], s52 offen offset:2048 lds\n"
    "v_mfma_f32_16x16x32_bf16 a[44:47], v[116:119], a[220:223], a[44:47]\n"
    "ds_read_b64_tr_b16 a[254:255], v244 offset:7168\n"
    "v_mfma_f32_16x16x32_bf16 a[44:47], v[112:115], a[216:219], a[44:47]\n"
    "ds_read_b64_tr_b16 a[192:193], v241 offset:16384\n"
    "v_mfma_f32_16x16x32_bf16 a[40:43], v[104:107], a[216:219], a[40:43]\n"
    "ds_read_b64_tr_b16 a[194:195], v241 offset:17408\n"
    "v_mfma_f32_16x16x32_bf16 a[40:43], v[108:111], a[220:223], a[40:43]\n"
    "ds_read_b64_tr_b16 a[196:197], v241 offset:18432\n"
    "v_mfma_f32_16x16x32_bf16 a[36:39], v[100:103], a[220:223], a[36:39]\n"
    "ds_read_b64_tr_b16 a[198:199], v241 offset:19456\n"
    "v_mfma_f32_16x16x32_bf16 a[36:39], v[96:99], a[216:219], a[36:39]\n"
    "s_waitcnt lgkmcnt(12)\n"
    "ds_read_b64_tr_b16 a[200:201], v242 offset:16384\n"
    "v_mfma_f32_16x16x32_bf16 a[48:51], v[96:99], a[224:227], a[48:51]\n"
    "buffer_load_dwordx4 v245, s[36:39], s52 offen offset:3072 lds\n"
    "v_mfma_f32_16x16x32_bf16 a[48:51], v[100:103], a[228:231], a[48:51]\n"
    "s_add_u32 m0, s55, 32768\n"
    "ds_read_b64_tr_b16 a[202:203], v242 offset:17408\n"
    "v_mfma_f32_16x16x32_bf16 a[52:55], v[108:111], a[228:231], a[52:55]\n"
    "ds_read_b64_tr_b16 a[204:205], v242 offset:18432\n"
    "v_mfma_f32_16x16x32_bf16 a[52:55], v[104:107], a[224:227], a[52:55]\n"
    "ds_read_b64_tr_b16 a[206:207], v242 offset:19456\n"
    "v_mfma_f32_16x16x32_bf16 a[56:59], v[112:115], a[224:227], a[56:59]\n"
    "ds_read_b64_tr_b16 a[208:209], v243 offset:16384\n"
    "v_mfma_f32_16x16x32_bf16 a[56:59], v[116:119], a[228:231], a[56:59]\n"
    "ds_read_b64_tr_b16 a[210:211], v243 offset:17408\n"
    "v_mfma_f32_16x16x32_bf16 a[68:71], v[116:119], a[236:239], a[68:71]\n"
    "ds_read_b64_tr_b16 a[212:213], v243 offset:18432\n"
    "v_mfma_f32_16x16x32_bf16 a[68:71], v[112:115], a[232:235], a[68:71]\n"
    "buffer_load_dwordx4 v245, s[36:39], s52 offen offset:128 lds\n"
    "v_mfma_f32_16x16x32_bf16 a[64:67], v[104:107], a[232:235], a[64:67]\n"
    "ds_read_b64_tr_b16 a[214:215], v243 offset:19456\n"
    "v_mfma_f32_16x16x32_bf16 a[64:67], v[108:111], a[236:239], a[64:67]\n"
    "ds_read_b64_tr_b16 a[216:217], v244 offset:16384\n"
    "v_mfma_f32_16x16x32_bf16 a[60:63], v[100:103], a[236:239], a[60:63]\n"
    "ds_read_b64_tr_b16 a[218:219], v244 offset:17408\n"
    "v_mfma_f32_16x16x32_bf16 a[60:63], v[96:99], a[232:235], a[60:63]\n"
    "s_waitcnt lgkmcnt(14)\n"
    "ds_read_b64_tr_b16 a[220:221], v244 offset:18432\n"
    "v_mfma_f32_16x16x32_bf16 a[72:75], v[96:99], a[240:243], a[72:75]\n"
    "ds_read_b64_tr_b16 a[222:223], v244 offset:19456\n"
    "v_mfma_f32_16x16x32_bf16 a[72:75], v[100:103], a[244:247], a[72:75]\n"
    "ds_read_b64_tr_b16 a[224:225], v241 offset:20480\n"
    "v_mfma_f32_16x16x32_bf16 a[76:79], v[108:111], a[244:247], a[76:79]\n"
    "buffer_load_dwordx4 v245, s[36:39], s52 offen offset:1152 lds\n"
    "v_mfma_f32_16x16x32_bf16 a[76:79], v[104:107], a[240:243], a[76:79]\n"
    "ds_read_b64_tr_b16 a[226:227], v241 offset:21504\n"
    "v_mfma_f32_16x16x32_bf16 a[80:83], v[112:115], a[240:243], a[80:83]\n"
    "ds_read_b64_tr_b16 a[228:229], v241 offset:22528\n"
    "v_mfma_f32_16x16x32_bf16 a[80:83], v[116:119], a[244:247], a[80:83]\n"
    "ds_read_b64_tr_b16 a[230:231], v241 offset:23552\n"
    "v_mfma_f32_16x16x32_bf16 a[92:95], v[116:119], a[252:255], a[92:95]\n"
    "ds_read_b64_tr_b16 a[232:233], v242 offset:20480\n"
    "v_mfma_f32_16x16x32_bf16 a[92:95], v[112:115], a[248:251], a[92:95]\n"
    "ds_read_b64_tr_b16 a[234:235], v242 offset:21504\n"
    "v_mfma_f32_16x16x32_bf16 a[88:91], v[104:107], a[248:251], a[88:91]\n"
    "v_exp_f32_e32 v24, v48\n"
    "v_mfma_f32_16x16x32_bf16 a[88:91], v[108:111], a[252:255], a[88:91]\n"
    "buffer_load_dwordx4 v245, s[36:39], s52 offen offset:2176 lds\n"
    "v_mfma_f32_16x16x32_bf16 a[84:87], v[100:103], a[252:255], a[84:87]\n"
    "v_exp_f32_e32 v25, v49\n"
    "v_mfma_f32_16x16x32_bf16 a[84:87], v[96:99], a[248:251], a[84:87]\n"
    "s_waitcnt lgkmcnt(14)\n"
    "ds_read_b64_tr_b16 a[236:237], v242 offset:22528\n"
    "v_mfma_f32_16x16x32_bf16 a[96:99], v[96:99], a[192:195], a[96:99]\n"
    "v_exp_f32_e32 v26, v50\n"
    "v_mfma_f32_16x16x32_bf16 a[96:99], v[100:103], a[196:199], a[96:99]\n"
    "v_exp_f32_e32 v27, v51\n"
    "v_mfma_f32_16x16x32_bf16 a[100:103], v[108:111], a[196:199], a[100:103]\n"
    "ds_read_b64_tr_b16 a[238:239], v242 offset:23552\n"
    "v_add_f32_e32 v234, v24, v25\n"
    "v_mfma_f32_16x16x32_bf16 a[100:103], v[104:107], a[192:195], a[100:103]\n"
    "v_exp_f32_e32 v28, v52\n"
    "v_mfma_f32_16x16x32_bf16 a[104:107], v[112:115], a[192:195], a[104:107]\n"
    "buffer_load_dwordx4 v245, s[36:39], s52 offen offset:3200 lds\n"
    "v_mfma_f32_16x16x32_bf16 a[104:107], v[116:119], a[196:199], a[104:107]\n"
    "ds_read_b64_tr_b16 a[240:241], v243 offset:20480\n"
    "v_add_f32_e32 v234, v26, v234\n"
    "v_mfma_f32_16x16x32_bf16 a[116:119], v[116:119], a[204:207], a[116:119]\n"
    "v_exp_f32_e32 v29, v53\n"
    "v_mfma_f32_16x16x32_bf16 a[116:119], v[112:115], a[200:203], a[116:119]\n"
    "ds_read_b64_tr_b16 a[242:243], v243 offset:21504\n"
    "v_add_f32_e32 v234, v27, v234\n"
    "v_mfma_f32_16x16x32_bf16 a[112:115], v[104:107], a[200:203], a[112:115]\n"
    "v_exp_f32_e32 v30, v54\n"
    "v_mfma_f32_16x16x32_bf16 a[112:115], v[108:111], a[204:207], a[112:115]\n"
    "ds_read_b64_tr_b16 a[244:245], v243 offset:22528\n"
    "v_add_f32_e32 v235, v28, v29\n"
    "v_mfma_f32_16x16x32_bf16 a[108:111], v[100:103], a[204:207], a[108:111]\n"
    "ds_read_b64_tr_b16 a[246:247], v243 offset:23552\n"
    "v_add_f32_e32 v235, v30, v235\n"
    "v_mfma_f32_16x16x32_bf16 a[108:111], v[96:99], a[200:203], a[108:111]\n"
    "s_waitcnt lgkmcnt(10)\n"
    "ds_read_b64_tr_b16 a[248:249], v244 offset:20480\n"
    "v_mfma_f32_16x16x32_bf16 a[120:123], v[96:99], a[208:211], a[120:123]\n"
    "v_exp_f32_e32 v31, v55\n"
    "v_mfma_f32_16x16x32_bf16 a[120:123], v[100:103], a[212:215], a[120:123]\n"
    "v_exp_f32_e32 v32, v56\n"
    "v_mfma_f32_16x16x32_bf16 a[124:127], v[108:111], a[212:215], a[124:127]\n"
    "ds_read_b64_tr_b16 a[250:251], v244 offset:21504\n"
    "v_add_f32_e32 v235, v31, v235\n"
    "v_mfma_f32_16x16x32_bf16 a[124:127], v[104:107], a[208:211], a[124:127]\n"
    "v_exp_f32_e32 v33, v57\n"
    "v_mfma_f32_16x16x32_bf16 a[128:131], v[112:115], a[208:211], a[128:131]\n"
    "ds_read_b64_tr_b16 a[252:253], v244 offset:22528\n"
    "v_add_f32_e32 v236, v32, v33\n"
    "v_mfma_f32_16x16x32_bf16 a[128:131], v[116:119], a[212:215], a[128:131]\n"
    "ds_read_b64_tr_b16 a[254:255], v244 offset:23552\n"
    "s_add_u32 s52, s52, 32768\n"
    "v_mfma_f32_16x16x32_bf16 a[140:143], v[116:119], a[220:223], a[140:143]\n"
    "v_exp_f32_e32 v34, v58\n"
    "v_mfma_f32_16x16x32_bf16 a[140:143], v[112:115], a[216:219], a[140:143]\n"
    "v_exp_f32_e32 v35, v59\n"
    "v_mfma_f32_16x16x32_bf16 a[136:139], v[104:107], a[216:219], a[136:139]\n"
    "v_exp_f32_e32 v36, v60\n"
    "v_mfma_f32_16x16x32_bf16 a[136:139], v[108:111], a[220:223], a[136:139]\n"
    "v_exp_f32_e32 v37, v61\n"
    "v_mfma_f32_16x16x32_bf16 a[132:135], v[100:103], a[220:223], a[132:135]\n"
    "v_exp_f32_e32 v38, v62\n"
    "v_mfma_f32_16x16x32_bf16 a[132:135], v[96:99], a[216:219], a[132:135]\n"
    "v_exp_f32_e32 v39, v63\n"
    "v_mfma_f32_16x16x32_bf16 a[144:147], v[96:99], a[224:227], a[144:147]\n"
    "s_waitcnt vmcnt(16) lgkmcnt(0)\n"
    "s_barrier\n"
    "v_mfma_f32_16x16x32_bf16 a[144:147], v[100:103], a[228:231], a[144:147]\n"
    "ds_read_b128 a[192:195], v240 offset:0\n"
    "v_add_f32_e32 v236, v34, v236\n"
    "v_mfma_f32_16x16x32_bf16 a[148:151], v[108:111], a[228:231], a[148:151]\n"
    "ds_read_b128 a[196:199], v240 offset:512\n"
    "v_add_f32_e32 v236, v35, v236\n"
    "v_mfma_f32_16x16x32_bf16 a[148:151], v[104:107], a[224:227], a[148:151]\n"
    "ds_read_b128 a[200:203], v240 offset:4096\n"
    "v_add_f32_e32 v234, v36, v234\n"
    "v_mfma_f32_16x16x32_bf16 a[152:155], v[112:115], a[224:227], a[152:155]\n"
    "ds_read_b128 a[204:207], v240 offset:4608\n"
    "v_add_f32_e32 v234, v37, v234\n"
    "v_mfma_f32_16x16x32_bf16 a[152:155], v[116:119], a[228:231], a[152:155]\n"
    "ds_read_b128 a[208:211], v240 offset:16384\n"
    "v_add_f32_e32 v234, v38, v234\n"
    "v_mfma_f32_16x16x32_bf16 a[164:167], v[116:119], a[236:239], a[164:167]\n"
    "ds_read_b128 a[212:215], v240 offset:16896\n"
    "v_add_f32_e32 v234, v39, v234\n"
    "v_mfma_f32_16x16x32_bf16 a[164:167], v[112:115], a[232:235], a[164:167]\n"
    "ds_read_b128 a[216:219], v240 offset:20480\n"
    "v_mfma_f32_16x16x32_bf16 a[160:163], v[104:107], a[232:235], a[160:163]\n"
    "ds_read_b128 a[220:223], v240 offset:20992\n"
    "v_mfma_f32_16x16x32_bf16 a[160:163], v[108:111], a[236:239], a[160:163]\n"
    "v_exp_f32_e32 v40, v64\n"
    "v_mfma_f32_16x16x32_bf16 a[156:159], v[100:103], a[236:239], a[156:159]\n"
    "v_exp_f32_e32 v41, v65\n"
    "v_mfma_f32_16x16x32_bf16 a[156:159], v[96:99], a[232:235], a[156:159]\n"
    "v_exp_f32_e32 v42, v66\n"
    "v_mfma_f32_16x16x32_bf16 a[168:171], v[96:99], a[240:243], a[168:171]\n"
    "v_exp_f32_e32 v43, v67\n"
    "v_mfma_f32_16x16x32_bf16 a[168:171], v[100:103], a[244:247], a[168:171]\n"
    "v_exp_f32_e32 v44, v68\n"
    "v_mfma_f32_16x16x32_bf16 a[172:175], v[108:111], a[244:247], a[172:175]\n"
    "v_exp_f32_e32 v45, v69\n"
    "v_mfma_f32_16x16x32_bf16 a[172:175], v[104:107], a[240:243], a[172:175]\n"
    "v_add_f32_e32 v235, v40, v235\n"
    "v_add_f32_e32 v235, v41, v235\n"
    "v_mfma_f32_16x16x32_bf16 a[176:179], v[112:115], a[240:243], a[176:179]\n"
    "v_add_f32_e32 v235, v42, v235\n"
    "v_add_f32_e32 v235, v43, v235\n"
    "v_mfma_f32_16x16x32_bf16 a[176:179], v[116:119], a[244:247], a[176:179]\n"
    "v_add_f32_e32 v236, v44, v236\n"
    "v_add_f32_e32 v236, v45, v236\n"
    "v_mfma_f32_16x16x32_bf16 a[188:191], v[116:119], a[252:255], a[188:191]\n"
    "v_mfma_f32_16x16x32_bf16 a[188:191], v[112:115], a[248:251], a[188:191]\n"
    "v_mfma_f32_16x16x32_bf16 a[184:187], v[104:107], a[248:251], a[184:187]\n"
    "v_mfma_f32_16x16x32_bf16 a[184:187], v[108:111], a[252:255], a[184:187]\n"
    "v_mfma_f32_16x16x32_bf16 a[180:183], v[100:103], a[252:255], a[180:183]\n"
    "v_mfma_f32_16x16x32_bf16 a[180:183], v[96:99], a[248:251], a[180:183]\n"
    "s_waitcnt lgkmcnt(4)\n"
    "v_mfma_f32_16x16x32_bf16 v[0:3], a[192:195], v[120:123], v[216:219]\n"
    "v_exp_f32_e32 v228, v70\n"
    "v_mfma_f32_16x16x32_bf16 v[0:3], a[196:199], v[124:127], v[0:3]\n"
    "v_exp_f32_e32 v229, v71\n"
    "v_mfma_f32_16x16x32_bf16 v[0:3], a[200:203], v[128:131], v[0:3]\n"
    "s_add_u32 m0, s54, 65536\n"
    "v_add_f32_e32 v236, v228, v236\n"
    "v_mfma_f32_16x16x32_bf16 v[0:3], a[204:207], v[132:135], v[0:3]\n"
    "s_waitcnt lgkmcnt(0)\n"
    "v_cvt_pk_bf16_f32 v96, v24, v25\n"
    "v_mfma_f32_16x16x32_bf16 v[0:3], a[208:211], v[136:139], v[0:3]\n"
    "buffer_load_dwordx4 v246, s[40:43], s53 offen offset:0 nt lds\n"
    "v_mfma_f32_16x16x32_bf16 v[0:3], a[212:215], v[140:143], v[0:3]\n"
    "ds_read_b128 a[224:227], v240 offset:1024\n"
    "v_add_f32_e32 v236, v229, v236\n"
    "v_mfma_f32_16x16x32_bf16 v[0:3], a[216:219], v[144:147], v[0:3]\n"
    "ds_read_b128 a[228:231], v240 offset:1536\n"
    "v_cvt_pk_bf16_f32 v115, v228, v229\n"
    "v_mfma_f32_16x16x32_bf16 v[0:3], a[220:223], v[148:151], v[0:3]\n"
    "ds_read_b128 a[232:235], v240 offset:5120\n"
    "v_cvt_pk_bf16_f32 v97, v26, v27\n"
    "v_mfma_f32_16x16x32_bf16 v[4:7], a[220:223], v[180:183], v[220:223]\n"
    "ds_read_b128 a[236:239], v240 offset:5632\n"
    "v_cvt_pk_bf16_f32 v104, v28, v29\n"
    "v_mfma_f32_16x16x32_bf16 v[4:7], a[216:219], v[176:179], v[4:7]\n"
    "v_exp_f32_e32 v230, v72\n"
    "v_mfma_f32_16x16x32_bf16 v[4:7], a[212:215], v[172:175], v[4:7]\n"
    "ds_read_b128 a[240:243], v240 offset:17408\n"
    "v_add_f32_e32 v234, v230, v234\n"
    "v_mfma_f32_16x16x32_bf16 v[4:7], a[208:211], v[168:171], v[4:7]\n"
    "buffer_load_dwordx4 v246, s[40:43], s53 offen offset:1024 nt lds\n"
    "v_mfma_f32_16x16x32_bf16 v[4:7], a[204:207], v[164:167], v[4:7]\n"
    "ds_read_b128 a[244:247], v240 offset:17920\n"
    "v_cvt_pk_bf16_f32 v112, v32, v33\n"
    "v_mfma_f32_16x16x32_bf16 v[4:7], a[200:203], v[160:163], v[4:7]\n"
    "ds_read_b128 a[248:251], v240 offset:21504\n"
    "v_cvt_pk_bf16_f32 v105, v30, v31\n"
    "v_mfma_f32_16x16x32_bf16 v[4:7], a[196:199], v[156:159], v[4:7]\n"
    "ds_read_b128 a[252:255], v240 offset:22016\n"
    "v_cvt_pk_bf16_f32 v113, v34, v35\n"
    "v_mfma_f32_16x16x32_bf16 v[4:7], a[192:195], v[152:155], v[4:7]\n"
    "v_exp_f32_e32 v231, v73\n"
    "v_mfma_f32_16x16x32_bf16 v[8:11], a[192:195], v[184:187], v[224:227]\n"
    "v_exp_f32_e32 v232, v74\n"
    "v_mfma_f32_16x16x32_bf16 v[8:11], a[196:199], v[188:191], v[8:11]\n"
    "buffer_load_dwordx4 v246, s[40:43], s53 offen offset:2048 nt lds\n"
    "v_mfma_f32_16x16x32_bf16 v[8:11], a[200:203], v[192:195], v[8:11]\n"
    "v_exp_f32_e32 v233, v75\n"
    "v_mfma_f32_16x16x32_bf16 v[8:11], a[204:207], v[196:199], v[8:11]\n"
    "v_add_f32_e32 v234, v231, v234\n"
    "v_cvt_pk_bf16_f32 v100, v230, v231\n"
    "v_mfma_f32_16x16x32_bf16 v[8:11], a[208:211], v[200:203], v[8:11]\n"
    "v_exp_f32_e32 v228, v76\n"
    "v_mfma_f32_16x16x32_bf16 v[8:11], a[212:215], v[204:207], v[8:11]\n"
    "v_exp_f32_e32 v229, v77\n"
    "v_mfma_f32_16x16x32_bf16 v[8:11], a[216:219], v[208:211], v[8:11]\n"
    "v_add_f32_e32 v234, v232, v234\n"
    "v_add_f32_e32 v234, v233, v234\n"
    "v_mfma_f32_16x16x32_bf16 v[8:11], a[220:223], v[212:215], v[8:11]\n"
    "s_waitcnt lgkmcnt(0)\n"
    "v_cvt_pk_bf16_f32 v101, v232, v233\n"
    "v_mfma_f32_16x16x32_bf16 v[20:23], a[252:255], v[212:215], v[224:227]\n"
    "buffer_load_dwordx4 v246, s[40:43], s53 offen offset:3072 nt lds\n"
    "v_mfma_f32_16x16x32_bf16 v[20:23], a[248:251], v[208:211], v[20:23]\n"
    "ds_read_b128 a[192:195], v240 offset:2048\n"
    "v_add_f32_e32 v235, v228, v235\n"
    "v_mfma_f32_16x16x32_bf16 v[20:23], a[244:247], v[204:207], v[20:23]\n"
    "ds_read_b128 a[196:199], v240 offset:2560\n"
    "v_add_f32_e32 v235, v229, v235\n"
    "v_mfma_f32_16x16x32_bf16 v[20:23], a[240:243], v[200:203], v[20:23]\n"
    "ds_read_b128 a[200:203], v240 offset:6144\n"
    "v_cvt_pk_bf16_f32 v108, v228, v229\n"
    "v_mfma_f32_16x16x32_bf16 v[20:23], a[236:239], v[196:199], v[20:23]\n"
    "v_exp_f32_e32 v232, v80\n"
    "v_mfma_f32_16x16x32_bf16 v[20:23], a[232:235], v[192:195], v[20:23]\n"
    "ds_read_b128 a[204:207], v240 offset:6656\n"
    "v_add_f32_e32 v236, v232, v236\n"
    "v_mfma_f32_16x16x32_bf16 v[20:23], a[228:231], v[188:191], v[20:23]\n"
    "s_add_u32 m0, s55, 65536\n"
    "ds_read_b128 a[208:211], v240 offset:18432\n"
    "v_mfma_f32_16x16x32_bf16 v[20:23], a[224:227], v[184:187], v[20:23]\n"
    "buffer_load_dwordx4 v246, s[40:43], s53 offen offset:128 nt lds\n"
    "v_mfma_f32_16x16x32_bf16 v[16:19], a[224:227], v[152:155], v[220:223]\n"
    "v_exp_f32_e32 v230, v78\n"
    "v_mfma_f32_16x16x32_bf16 v[16:19], a[228:231], v[156:159], v[16:19]\n"
    "v_exp_f32_e32 v231, v79\n"
    "v_mfma_f32_16x16x32_bf16 v[16:19], a[232:235], v[160:163], v[16:19]\n"
    "ds_read_b128 a[212:215], v240 offset:18944\n"
    "v_add_f32_e32 v235, v230, v235\n"
    "v_mfma_f32_16x16x32_bf16 v[16:19], a[236:239], v[164:167], v[16:19]\n"
    "v_exp_f32_e32 v233, v81\n"
    "v_mfma_f32_16x16x32_bf16 v[16:19], a[240:243], v[168:171], v[16:19]\n"
    "ds_read_b128 a[216:219], v240 offset:22528\n"
    "v_add_f32_e32 v235, v231, v235\n"
    "v_mfma_f32_16x16x32_bf16 v[16:19], a[244:247], v[172:175], v[16:19]\n"
    "ds_read_b128 a[220:223], v240 offset:23040\n"
    "v_cvt_pk_bf16_f32 v109, v230, v231\n"
    "v_mfma_f32_16x16x32_bf16 v[16:19], a[248:251], v[176:179], v[16:19]\n"
    "buffer_load_dwordx4 v246, s[40:43], s53 offen offset:1152 nt lds\n"
    "v_mfma_f32_16x16x32_bf16 v[16:19], a[252:255], v[180:183], v[16:19]\n"
    "v_exp_f32_e32 v228, v82\n"
    "v_mfma_f32_16x16x32_bf16 v[12:15], a[252:255], v[148:151], v[216:219]\n"
    "v_exp_f32_e32 v229, v83\n"
    "v_mfma_f32_16x16x32_bf16 v[12:15], a[248:251], v[144:147], v[12:15]\n"
    "v_exp_f32_e32 v230, v84\n"
    "v_mfma_f32_16x16x32_bf16 v[12:15], a[244:247], v[140:143], v[12:15]\n"
    "v_cvt_pk_bf16_f32 v116, v232, v233\n"
    "v_add_f32_e32 v236, v233, v236\n"
    "v_mfma_f32_16x16x32_bf16 v[12:15], a[240:243], v[136:139], v[12:15]\n"
    "buffer_load_dwordx4 v246, s[40:43], s53 offen offset:2176 nt lds\n"
    "v_mfma_f32_16x16x32_bf16 v[12:15], a[236:239], v[132:135], v[12:15]\n"
    "v_exp_f32_e32 v231, v85\n"
    "v_mfma_f32_16x16x32_bf16 v[12:15], a[232:235], v[128:131], v[12:15]\n"
    "v_exp_f32_e32 v232, v86\n"
    "v_mfma_f32_16x16x32_bf16 v[12:15], a[228:231], v[124:127], v[12:15]\n"
    "v_cvt_pk_bf16_f32 v98, v36, v37\n"
    "v_add_f32_e32 v236, v228, v236\n"
    "v_mfma_f32_16x16x32_bf16 v[12:15], a[224:227], v[120:123], v[12:15]\n"
    "s_waitcnt lgkmcnt(0)\n"
    "v_add_f32_e32 v236, v229, v236\n"
    "v_mfma_f32_16x16x32_bf16 v[24:27], a[192:195], v[120:123], v[216:219]\n"
    "ds_read_b128 a[224:227], v240 offset:3072\n"
    "v_cvt_pk_bf16_f32 v117, v228, v229\n"
    "v_mfma_f32_16x16x32_bf16 v[24:27], a[196:199], v[124:127], v[24:27]\n"
    "ds_read_b128 a[228:231], v240 offset:3584\n"
    "v_cvt_pk_bf16_f32 v99, v38, v39\n"
    "v_mfma_f32_16x16x32_bf16 v[24:27], a[200:203], v[128:131], v[24:27]\n"
    "ds_read_b128 a[232:235], v240 offset:7168\n"
    "v_cvt_pk_bf16_f32 v102, v230, v231\n"
    "v_mfma_f32_16x16x32_bf16 v[24:27], a[204:207], v[132:135], v[24:27]\n"
    "ds_read_b128 a[236:239], v240 offset:7680\n"
    "v_add_f32_e32 v234, v230, v234\n"
    "v_mfma_f32_16x16x32_bf16 v[24:27], a[208:211], v[136:139], v[24:27]\n"
    "ds_read_b128 a[240:243], v240 offset:19456\n"
    "v_add_f32_e32 v234, v231, v234\n"
    "v_mfma_f32_16x16x32_bf16 v[24:27], a[212:215], v[140:143], v[24:27]\n"
    "buffer_load_dwordx4 v246, s[40:43], s53 offen offset:3200 nt lds\n"
    "v_mfma_f32_16x16x32_bf16 v[24:27], a[216:219], v[144:147], v[24:27]\n"
    "ds_read_b128 a[244:247], v240 offset:19968\n"
    "v_add_f32_e32 v234, v232, v234\n"
    "v_mfma_f32_16x16x32_bf16 v[24:27], a[220:223], v[148:151], v[24:27]\n"
    "ds_read_b128 a[248:251], v240 offset:23552\n"
    "v_cvt_pk_bf16_f32 v106, v40, v41\n"
    "v_mfma_f32_16x16x32_bf16 v[28:31], a[220:223], v[180:183], v[220:223]\n"
    "ds_read_b128 a[252:255], v240 offset:24064\n"
    "v_cvt_pk_bf16_f32 v107, v42, v43\n"
    "v_mfma_f32_16x16x32_bf16 v[28:31], a[216:219], v[176:179], v[28:31]\n"
    "v_exp_f32_e32 v233, v87\n"
    "v_mfma_f32_16x16x32_bf16 v[28:31], a[212:215], v[172:175], v[28:31]\n"
    "v_exp_f32_e32 v230, v90\n"
    "v_mfma_f32_16x16x32_bf16 v[28:31], a[208:211], v[168:171], v[28:31]\n"
    "v_exp_f32_e32 v228, v88\n"
    "v_mfma_f32_16x16x32_bf16 v[28:31], a[204:207], v[164:167], v[28:31]\n"
    "v_exp_f32_e32 v229, v89\n"
    "v_mfma_f32_16x16x32_bf16 v[28:31], a[200:203], v[160:163], v[28:31]\n"
    "v_exp_f32_e32 v231, v91\n"
    "v_mfma_f32_16x16x32_bf16 v[28:31], a[196:199], v[156:159], v[28:31]\n"
    "v_add_f32_e32 v234, v233, v234\n"
    "v_add_f32_e32 v235, v228, v235\n"
    "v_mfma_f32_16x16x32_bf16 v[28:31], a[192:195], v[152:155], v[28:31]\n"
    "v_cvt_pk_bf16_f32 v103, v232, v233\n"
    "v_add_f32_e32 v235, v229, v235\n"
    "v_mfma_f32_16x16x32_bf16 v[32:35], a[192:195], v[184:187], v[224:227]\n"
    "v_exp_f32_e32 v232, v92\n"
    "v_mfma_f32_16x16x32_bf16 v[32:35], a[196:199], v[188:191], v[32:35]\n"
    "s_add_u32 s53, s53, 32768\n"
    "v_add_f32_e32 v235, v230, v235\n"
    "v_mfma_f32_16x16x32_bf16 v[32:35], a[200:203], v[192:195], v[32:35]\n"
    "v_exp_f32_e32 v233, v93\n"
    "v_mfma_f32_16x16x32_bf16 v[32:35], a[204:207], v[196:199], v[32:35]\n"
    "v_cvt_pk_bf16_f32 v110, v228, v229\n"
    "v_add_f32_e32 v235, v231, v235\n"
    "v_mfma_f32_16x16x32_bf16 v[32:35], a[208:211], v[200:203], v[32:35]\n"
    "v_cvt_pk_bf16_f32 v114, v44, v45\n"
    "v_cvt_pk_bf16_f32 v111, v230, v231\n"
    "v_mfma_f32_16x16x32_bf16 v[32:35], a[212:215], v[204:207], v[32:35]\n"
    "v_exp_f32_e32 v228, v94\n"
    "v_mfma_f32_16x16x32_bf16 v[32:35], a[216:219], v[208:211], v[32:35]\n"
    "v_exp_f32_e32 v229, v95\n"
    "v_mfma_f32_16x16x32_bf16 v[32:35], a[220:223], v[212:215], v[32:35]\n"
    "s_waitcnt vmcnt(16) lgkmcnt(0)\n"
    "s_barrier\n"
    "v_mfma_f32_16x16x32_bf16 v[44:47], a[252:255], v[212:215], v[224:227]\n"
    "ds_read_b64_tr_b16 a[192:193], v241 offset:32768\n"
    "v_add_f32_e32 v236, v232, v236\n"
    "v_mfma_f32_16x16x32_bf16 v[44:47], a[248:251], v[208:211], v[44:47]\n"
    "ds_read_b64_tr_b16 a[194:195], v241 offset:33792\n"
    "v_cvt_pk_bf16_f32 v118, v232, v233\n"
    "v_mfma_f32_16x16x32_bf16 v[44:47], a[244:247], v[204:207], v[44:47]\n"
    "ds_read_b64_tr_b16 a[196:197], v241 offset:34816\n"
    "v_add_f32_e32 v236, v233, v236\n"
    "v_mfma_f32_16x16x32_bf16 v[44:47], a[240:243], v[200:203], v[44:47]\n"
    "ds_read_b64_tr_b16 a[198:199], v241 offset:35840\n"
    "v_add_f32_e32 v236, v228, v236\n"
    "v_mfma_f32_16x16x32_bf16 v[44:47], a[236:239], v[196:199], v[44:47]\n"
    "ds_read_b64_tr_b16 a[200:201], v242 offset:32768\n"
    "v_cvt_pk_bf16_f32 v119, v228, v229\n"
    "v_mfma_f32_16x16x32_bf16 v[44:47], a[232:235], v[192:195], v[44:47]\n"
    "ds_read_b64_tr_b16 a[202:203], v242 offset:33792\n"
    "v_add_f32_e32 v236, v229, v236\n"
    "v_mfma_f32_16x16x32_bf16 v[44:47], a[228:231], v[188:191], v[44:47]\n"
    "ds_read_b64_tr_b16 a[204:205], v242 offset:34816\n"
    "v_max3_f32 v251, v234, v235, v236\n"
    "v_mfma_f32_16x16x32_bf16 v[44:47], a[224:227], v[184:187], v[44:47]\n"
    "ds_read_b64_tr_b16 a[206:207], v242 offset:35840\n"
    "v_cmp_lt_f32_e32 vcc, s57, v251\n"
    "v_mfma_f32_16x16x32_bf16 v[40:43], a[224:227], v[152:155], v[220:223]\n"
    "ds_read_b64_tr_b16 a[208:209], v243 offset:32768\n"
    "v_mfma_f32_16x16x32_bf16 v[40:43], a[228:231], v[156:159], v[40:43]\n"
    "ds_read_b64_tr_b16 a[210:211], v243 offset:33792\n"
    "v_mfma_f32_16x16x32_bf16 v[40:43], a[232:235], v[160:163], v[40:43]\n"
    "ds_read_b64_tr_b16 a[212:213], v243 offset:34816\n"
    "s_cmp_eq_u32 s56, 1\n"
    "v_mfma_f32_16x16x32_bf16 v[40:43], a[236:239], v[164:167], v[40:43]\n"
    "ds_read_b64_tr_b16 a[214:215], v243 offset:35840\n"
    "v_mfma_f32_16x16x32_bf16 v[40:43], a[240:243], v[168:171], v[40:43]\n"
    "ds_read_b64_tr_b16 a[216:217], v244 offset:32768\n"
    "s_cbranch_scc1 VE_%=\n"
    "VER_%=:\n"
    "v_mfma_f32_16x16x32_bf16 v[40:43], a[244:247], v[172:175], v[40:43]\n"
    "ds_read_b64_tr_b16 a[218:219], v244 offset:33792\n"
    "v_mfma_f32_16x16x32_bf16 v[40:43], a[248:251], v[176:179], v[40:43]\n"
    "ds_read_b64_tr_b16 a[220:221], v244 offset:34816\n"
    "v_mfma_f32_16x16x32_bf16 v[40:43], a[252:255], v[180:183], v[40:43]\n"
    "ds_read_b64_tr_b16 a[222:223], v244 offset:35840\n"
    "v_mfma_f32_16x16x32_bf16 v[36:39], a[252:255], v[148:151], v[216:219]\n"
    "v_mfma_f32_16x16x32_bf16 v[36:39], a[248:251], v[144:147], v[36:39]\n"
    "v_mfma_f32_16x16x32_bf16 v[36:39], a[244:247], v[140:143], v[36:39]\n"
    "v_mfma_f32_16x16x32_bf16 v[36:39], a[240:243], v[136:139], v[36:39]\n"
    "v_mfma_f32_16x16x32_bf16 v[36:39], a[236:239], v[132:135], v[36:39]\n"
    "v_mfma_f32_16x16x32_bf16 v[36:39], a[232:235], v[128:131], v[36:39]\n"
    "v_mfma_f32_16x16x32_bf16 v[36:39], a[228:231], v[124:127], v[36:39]\n"
    "v_mfma_f32_16x16x32_bf16 v[36:39], a[224:227], v[120:123], v[36:39]\n"
    "s_waitcnt lgkmcnt(12)\n"
    "s_cbranch_vccnz RARE0_%=\n"
    "RET0_%=:\n"
    "v_mfma_f32_16x16x32_bf16 a[0:3], v[96:99], a[192:195], a[0:3]\n"
    "s_add_u32 m0, s54, 0\n"
    "ds_read_b64_tr_b16 a[224:225], v241 offset:36864\n"
    "v_mfma_f32_16x16x32_bf16 a[0:3], v[100:103], a[196:199], a[0:3]\n"
    "ds_read_b64_tr_b16 a[226:227], v241 offset:37888\n"
    "v_add_f32_e32 v238, v235, v238\n"
    "v_mfma_f32_16x16x32_bf16 a[4:7], v[108:111], a[196:199], a[4:7]\n"
    "ds_read_b64_tr_b16 a[228:229], v241 offset:38912\n"
    "v_add_f32_e32 v237, v234, v237\n"
    "v_mfma_f32_16x16x32_bf16 a[4:7], v[104:107], a[192:195], a[4:7]\n"
    "buffer_load_dwordx4 v245, s[36:39], s52 offen offset:0 lds\n"
    "v_mfma_f32_16x16x32_bf16 a[8:11], v[112:115], a[192:195], a[8:11]\n"
    "ds_read_b64_tr_b16 a[230:231], v241 offset:39936\n"
    "v_add_f32_e32 v239, v236, v239\n"
    "v_mfma_f32_16x16x32_bf16 a[8:11], v[116:119], a[196:199], a[8:11]\n"
    "s_waitcnt lgkmcnt(12)\n"
    "ds_read_b64_tr_b16 a[232:233], v242 offset:36864\n"
    "v_mfma_f32_16x16x32_bf16 a[20:23], v[116:119], a[204:207], a[20:23]\n"
    "ds_read_b64_tr_b16 a[234:235], v242 offset:37888\n"
    "v_mfma_f32_16x16x32_bf16 a[20:23], v[112:115], a[200:203], a[20:23]\n"
    "ds_read_b64_tr_b16 a[236:237], v242 offset:38912\n"
    "v_mfma_f32_16x16x32_bf16 a[16:19], v[104:107], a[200:203], a[16:19]\n"
    "ds_read_b64_tr_b16 a[238:239], v242 offset:39936\n"
    "v_mfma_f32_16x16x32_bf16 a[16:19], v[108:111], a[204:207], a[16:19]\n"
    "ds_read_b64_tr_b16 a[240:241], v243 offset:36864\n"
    "v_mfma_f32_16x16x32_bf16 a[12:15], v[100:103], a[204:207], a[12:15]\n"
    "buffer_load_dwordx4 v245, s[36:39], s52 offen offset:1024 lds\n"
    "v_mfma_f32_16x16x32_bf16 a[12:15], v[96:99], a[200:203], a[12:15]\n"
    "s_waitcnt lgkmcnt(9)\n"
    "ds_read_b64_tr_b16 a[242:243], v243 offset:37888\n"
    "v_mfma_f32_16x16x32_bf16 a[24:27], v[96:99], a[208:211], a[24:27]\n"
    "ds_read_b64_tr_b16 a[244:245], v243 offset:38912\n"
    "v_mfma_f32_16x16x32_bf16 a[24:27], v[100:103], a[212:215], a[24:27]\n"
    "ds_read_b64_tr_b16 a[246:247], v243 offset:39936\n"
    "v_mfma_f32_16x16x32_bf16 a[28:31], v[108:111], a[212:215], a[28:31]\n"
    "ds_read_b64_tr_b16 a[248:249], v244 offset:36864\n"
    "v_mfma_f32_16x16x32_bf16 a[28:31], v[104:107], a[208:211], a[28:31]\n"
    "ds_read_b64_tr_b16 a[250:251], v244 offset:37888\n"
    "v_mfma_f32_16x16x32_bf16 a[32:35], v[112:115], a[208:211], a[32:35]\n"
    "ds_read_b64_tr_b16 a[252:253], v244 offset:38912\n"
    "v_mfma_f32_16x16x32_bf16 a[32:35], v[116:119], a[212:215], a[32:35]\n"
    "buffer_load_dwordx4 v245, s[36:39], s52 offen offset:2048 lds\n"
    "v_mfma_f32_16x16x32_bf16 a[44:47], v[116:119], a[220:223], a[44:47]\n"
    "ds_read_b64_tr_b16 a[254:255], v244 offset:39936\n"
    "v_mfma_f32_16x16x32_bf16 a[44:47], v[112:115], a[216:219], a[44:47]\n"
    "ds_read_b64_tr_b16 a[192:193], v241 offset:49152\n"
    "v_mfma_f32_16x16x32_bf16 a[40:43], v[104:107], a[216:219], a[40:43]\n"
    "ds_read_b64_tr_b16 a[194:195], v241 offset:50176\n"
    "v_mfma_f32_16x16x32_bf16 a[40:43], v[108:111], a[220:223], a[40:43]\n"
    "ds_read_b64_tr_b16 a[196:197], v241 offset:51200\n"
    "v_mfma_f32_16x16x32_bf16 a[36:39], v[100:103], a[220:223], a[36:39]\n"
    "ds_read_b64_tr_b16 a[198:199], v241 offset:52224\n"
    "v_mfma_f32_16x16x32_bf16 a[36:39], v[96:99], a[216:219], a[36:39]\n"
    "s_waitcnt lgkmcnt(12)\n"
    "ds_read_b64_tr_b16 a[200:201], v242 offset:49152\n"
    "v_mfma_f32_16x16x32_bf16 a[48:51], v[96:99], a[224:227], a[48:51]\n"
    "buffer_load_dwordx4 v245, s[36:39], s52 offen offset:3072 lds\n"
    "v_mfma_f32_16x16x32_bf16 a[48:51], v[100:103], a[228:231], a[48:51]\n"
    "s_add_u32 m0, s55, 0\n"
    "ds_read_b64_tr_b16 a[202:203], v242 offset:50176\n"
    "v_mfma_f32_16x16x32_bf16 a[52:55], v[108:111], a[228:231], a[52:55]\n"
    "ds_read_b64_tr_b16 a[204:205], v242 offset:51200\n"
    "v_mfma_f32_16x16x32_bf16 a[52:55], v[104:107], a[224:227], a[52:55]\n"
    "ds_read_b64_tr_b16 a[206:207], v242 offset:52224\n"
    "v_mfma_f32_16x16x32_bf16 a[56:59], v[112:115], a[224:227], a[56:59]\n"
    "ds_read_b64_tr_b16 a[208:209], v243 offset:49152\n"
    "v_mfma_f32_16x16x32_bf16 a[56:59], v[116:119], a[228:231], a[56:59]\n"
    "ds_read_b64_tr_b16 a[210:211], v243 offset:50176\n"
    "v_mfma_f32_16x16x32_bf16 a[68:71], v[116:119], a[236:239], a[68:71]\n"
    "ds_read_b64_tr_b16 a[212:213], v243 offset:51200\n"
    "v_mfma_f32_16x16x32_bf16 a[68:71], v[112:115], a[232:235], a[68:71]\n"
    "buffer_load_dwordx4 v245, s[36:39], s52 offen offset:128 lds\n"
    "v_mfma_f32_16x16x32_bf16 a[64:67], v[104:107], a[232:235], a[64:67]\n"
    "ds_read_b64_tr_b16 a[214:215], v243 offset:52224\n"
    "v_mfma_f32_16x16x32_bf16 a[64:67], v[108:111], a[236:239], a[64:67]\n"
    "ds_read_b64_tr_b16 a[216:217], v244 offset:49152\n"
    "v_mfma_f32_16x16x32_bf16 a[60:63], v[100:103], a[236:239], a[60:63]\n"
    "ds_read_b64_tr_b16 a[218:219], v244 offset:50176\n"
    "v_mfma_f32_16x16x32_bf16 a[60:63], v[96:99], a[232:235], a[60:63]\n"
    "s_waitcnt lgkmcnt(14)\n"
    "ds_read_b64_tr_b16 a[220:221], v244 offset:51200\n"
    "v_mfma_f32_16x16x32_bf16 a[72:75], v[96:99], a[240:243], a[72:75]\n"
    "ds_read_b64_tr_b16 a[222:223], v244 offset:52224\n"
    "v_mfma_f32_16x16x32_bf16 a[72:75], v[100:103], a[244:247], a[72:75]\n"
    "ds_read_b64_tr_b16 a[224:225], v241 offset:53248\n"
    "v_mfma_f32_16x16x32_bf16 a[76:79], v[108:111], a[244:247], a[76:79]\n"
    "buffer_load_dwordx4 v245, s[36:39], s52 offen offset:1152 lds\n"
    "v_mfma_f32_16x16x32_bf16 a[76:79], v[104:107], a[240:243], a[76:79]\n"
    "ds_read_b64_tr_b16 a[226:227], v241 offset:54272\n"
    "v_mfma_f32_16x16x32_bf16 a[80:83], v[112:115], a[240:243], a[80:83]\n"
    "ds_read_b64_tr_b16 a[228:229], v241 offset:55296\n"
    "v_mfma_f32_16x16x32_bf16 a[80:83], v[116:119], a[244:247], a[80:83]\n"
    "ds_read_b64_tr_b16 a[230:231], v241 offset:56320\n"
    "v_mfma_f32_16x16x32_bf16 a[92:95], v[116:119], a[252:255], a[92:95]\n"
    "ds_read_b64_tr_b16 a[232:233], v242 offset:53248\n"
    "v_mfma_f32_16x16x32_bf16 a[92:95], v[112:115], a[248:251], a[92:95]\n"
    "ds_read_b64_tr_b16 a[234:235], v242 offset:54272\n"
    "v_mfma_f32_16x16x32_bf16 a[88:91], v[104:107], a[248:251], a[88:91]\n"
    "v_exp_f32_e32 v72, v0\n"
    "v_mfma_f32_16x16x32_bf16 a[88:91], v[108:111], a[252:255], a[88:91]\n"
    "buffer_load_dwordx4 v245, s[36:39], s52 offen offset:2176 lds\n"
    "v_mfma_f32_16x16x32_bf16 a[84:87], v[100:103], a[252:255], a[84:87]\n"
    "v_exp_f32_e32 v73, v1\n"
    "v_mfma_f32_16x16x32_bf16 a[84:87], v[96:99], a[248:251], a[84:87]\n"
    "s_waitcnt lgkmcnt(14)\n"
    "ds_read_b64_tr_b16 a[236:237], v242 offset:55296\n"
    "v_mfma_f32_16x16x32_bf16 a[96:99], v[96:99], a[192:195], a[96:99]\n"
    "v_exp_f32_e32 v74, v2\n"
    "v_mfma_f32_16x16x32_bf16 a[96:99], v[100:103], a[196:199], a[96:99]\n"
    "v_exp_f32_e32 v75, v3\n"
    "v_mfma_f32_16x16x32_bf16 a[100:103], v[108:111], a[196:199], a[100:103]\n"
    "ds_read_b64_tr_b16 a[238:239], v242 offset:56320\n"
    "v_add_f32_e32 v234, v72, v73\n"
    "v_mfma_f32_16x16x32_bf16 a[100:103], v[104:107], a[192:195], a[100:103]\n"
    "v_exp_f32_e32 v76, v4\n"
    "v_mfma_f32_16x16x32_bf16 a[104:107], v[112:115], a[192:195], a[104:107]\n"
    "buffer_load_dwordx4 v245, s[36:39], s52 offen offset:3200 lds\n"
    "v_mfma_f32_16x16x32_bf16 a[104:107], v[116:119], a[196:199], a[104:107]\n"
    "ds_read_b64_tr_b16 a[240:241], v243 offset:53248\n"
    "v_add_f32_e32 v234, v74, v234\n"
    "v_mfma_f32_16x16x32_bf16 a[116:119], v[116:119], a[204:207], a[116:119]\n"
    "v_exp_f32_e32 v77, v5\n"
    "v_mfma_f32_16x16x32_bf16 a[116:119], v[112:115], a[200:203], a[116:119]\n"
    "ds_read_b64_tr_b16 a[242:243], v243 offset:54272\n"
    "v_add_f32_e32 v234, v75, v234\n"
    "v_mfma_f32_16x16x32_bf16 a[112:115], v[104:107], a[200:203], a[112:115]\n"
    "v_exp_f32_e32 v78, v6\n"
    "v_mfma_f32_16x16x32_bf16 a[112:115], v[108:111], a[204:207], a[112:115]\n"
    "ds_read_b64_tr_b16 a[244:245], v243 offset:55296\n"
    "v_add_f32_e32 v235, v76, v77\n"
    "v_mfma_f32_16x16x32_bf16 a[108:111], v[100:103], a[204:207], a[108:111]\n"
    "ds_read_b64_tr_b16 a[246:247], v243 offset:56320\n"
    "v_add_f32_e32 v235, v78, v235\n"
    "v_mfma_f32_16x16x32_bf16 a[108:111], v[96:99], a[200:203], a[108:111]\n"
    "s_waitcnt lgkmcnt(10)\n"
    "ds_read_b64_tr_b16 a[248:249], v244 offset:53248\n"
    "v_mfma_f32_16x16x32_bf16 a[120:123], v[96:99], a[208:211], a[120:123]\n"
    "v_exp_f32_e32 v79, v7\n"
    "v_mfma_f32_16x16x32_bf16 a[120:123], v[100:103], a[212:215], a[120:123]\n"
    "v_exp_f32_e32 v80, v8\n"
    "v_mfma_f32_16x16x32_bf16 a[124:127], v[108:111], a[212:215], a[124:127]\n"
    "ds_read_b64_tr_b16 a[250:251], v244 offset:54272\n"
    "v_add_f32_e32 v235, v79, v235\n"
    "v_mfma_f32_16x16x32_bf16 a[124:127], v[104:107], a[208:211], a[124:127]\n"
    "v_exp_f32_e32 v81, v9\n"
    "v_mfma_f32_16x16x32_bf16 a[128:131], v[112:115], a[208:211], a[128:131]\n"
    "ds_read_b64_tr_b16 a[252:253], v244 offset:55296\n"
    "v_add_f32_e32 v236, v80, v81\n"
    "v_mfma_f32_16x16x32_bf16 a[128:131], v[116:119], a[212:215], a[128:131]\n"
    "ds_read_b64_tr_b16 a[254:255], v244 offset:56320\n"
    "s_add_u32 s52, s52, 32768\n"
    "v_mfma_f32_16x16x32_bf16 a[140:143], v[116:119], a[220:223], a[140:143]\n"
    "v_exp_f32_e32 v82, v10\n"
    "v_mfma_f32_16x16x32_bf16 a[140:143], v[112:115], a[216:219], a[140:143]\n"
    "v_exp_f32_e32 v83, v11\n"
    "v_mfma_f32_16x16x32_bf16 a[136:139], v[104:107], a[216:219], a[136:139]\n"
    "v_exp_f32_e32 v84, v12\n"
    "v_mfma_f32_16x16x32_bf16 a[136:139], v[108:111], a[220:223], a[136:139]\n"
    "v_exp_f32_e32 v85, v13\n"
    "v_mfma_f32_16x16x32_bf16 a[132:135], v[100:103], a[220:223], a[132:135]\n"
    "v_exp_f32_e32 v86, v14\n"
    "v_mfma_f32_16x16x32_bf16 a[132:135], v[96:99], a[216:219], a[132:135]\n"
    "v_exp_f32_e32 v87, v15\n"
    "v_mfma_f32_16x16x32_bf16 a[144:147], v[96:99], a[224:227], a[144:147]\n"
    "s_waitcnt vmcnt(16) lgkmcnt(0)\n"
    "s_barrier\n"
    "v_mfma_f32_16x16x32_bf16 a[144:147], v[100:103], a[228:231], a[144:147]\n"
    "ds_read_b128 a[192:195], v240 offset:32768\n"
    "v_add_f32_e32 v236, v82, v236\n"
    "v_mfma_f32_16x16x32_bf16 a[148:151], v[108:111], a[228:231], a[148:151]\n"
    "ds_read_b128 a[196:199], v240 offset:33280\n"
    "v_add_f32_e32 v236, v83, v236\n"
    "v_mfma_f32_16x16x32_bf16 a[148:151], v[104:107], a[224:227], a[148:151]\n"
    "ds_read_b128 a[200:203], v240 offset:36864\n"
    "v_add_f32_e32 v234, v84, v234\n"
    "v_mfma_f32_16x16x32_bf16 a[152:155], v[112:115], a[224:227], a[152:155]\n"
    "ds_read_b128 a[204:207], v240 offset:37376\n"
    "v_add_f32_e32 v234, v85, v234\n"
    "v_mfma_f32_16x16x32_bf16 a[152:155], v[116:119], a[228:231], a[152:155]\n"
    "ds_read_b128 a[208:211], v240 offset:49152\n"
    "v_add_f32_e32 v234, v86, v234\n"
    "v_mfma_f32_16x16x32_bf16 a[164:167], v[116:119], a[236:239], a[164:167]\n"
    "ds_read_b128 a[212:215], v240 offset:49664\n"
    "v_add_f32_e32 v234, v87, v234\n"
    "v_mfma_f32_16x16x32_bf16 a[164:167], v[112:115], a[232:235], a[164:167]\n"
    "ds_read_b128 a[216:219], v240 offset:53248\n"
    "v_mfma_f32_16x16x32_bf16 a[160:163], v[104:107], a[232:235], a[160:163]\n"
    "ds_read_b128 a[220:223], v240 offset:53760\n"
    "v_mfma_f32_16x16x32_bf16 a[160:163], v[108:111], a[236:239], a[160:163]\n"
    "v_exp_f32_e32 v88, v16\n"
    "v_mfma_f32_16x16x32_bf16 a[156:159], v[100:103], a[236:239], a[156:159]\n"
    "v_exp_f32_e32 v89, v17\n"
    "v_mfma_f32_16x16x32_bf16 a[156:159], v[96:99], a[232:235], a[156:159]\n"
    "v_exp_f32_e32 v90, v18\n"
    "v_mfma_f32_16x16x32_bf16 a[168:171], v[96:99], a[240:243], a[168:171]\n"
    "v_exp_f32_e32 v91, v19\n"
    "v_mfma_f32_16x16x32_bf16 a[168:171], v[100:103], a[244:247], a[168:171]\n"
    "v_exp_f32_e32 v92, v20\n"
    "v_mfma_f32_16x16x32_bf16 a[172:175], v[108:111], a[244:247], a[172:175]\n"
    "v_exp_f32_e32 v93, v21\n"
    "v_mfma_f32_16x16x32_bf16 a[172:175], v[104:107], a[240:243], a[172:175]\n"
    "v_add_f32_e32 v235, v88, v235\n"
    "v_add_f32_e32 v235, v89, v235\n"
    "v_mfma_f32_16x16x32_bf16 a[176:179], v[112:115], a[240:243], a[176:179]\n"
    "v_add_f32_e32 v235, v90, v235\n"
    "v_add_f32_e32 v235, v91, v235\n"
    "v_mfma_f32_16x16x32_bf16 a[176:179], v[116:119], a[244:247], a[176:179]\n"
    "v_add_f32_e32 v236, v92, v236\n"
    "v_add_f32_e32 v236, v93, v236\n"
    "v_mfma_f32_16x16x32_bf16 a[188:191], v[116:119], a[252:255], a[188:191]\n"
    "v_mfma_f32_16x16x32_bf16 a[188:191], v[112:115], a[248:251], a[188:191]\n"
    "v_mfma_f32_16x16x32_bf16 a[184:187], v[104:107], a[248:251], a[184:187]\n"
    "v_mfma_f32_16x16x32_bf16 a[184:187], v[108:111], a[252:255], a[184:187]\n"
    "v_mfma_f32_16x16x32_bf16 a[180:183], v[100:103], a[252:255], a[180:183]\n"
    "v_mfma_f32_16x16x32_bf16 a[180:183], v[96:99], a[248:251], a[180:183]\n"
    "s_waitcnt lgkmcnt(4)\n"
    "s_branch LOOP_%=\n"
    "VE_%=:\n"
    "s_mov_b32 s36, s40\n"
    "s_mov_b32 s37, s41\n"
    "s_mov_b32 s52, 1015808\n"
    "s_mov_b32 s53, 1048576\n"
    "v_mov_b32_e32 v245, v246\n"
    "s_branch VER_%=\n"
    "RARE0_%=:\n"
    "s_nop 7\n"
    "s_waitcnt lgkmcnt(0)\n"
    "v_max3_f32 v251, v48, v49, v50\n"
    "v_max3_f32 v251, v251, v51, v60\n"
    "v_max3_f32 v251, v251, v61, v62\n"
    "v_max3_f32 v251, v251, v63, v72\n"
    "v_max3_f32 v251, v251, v73, v74\n"
    "v_max3_f32 v251, v251, v75, v84\n"
    "v_max3_f32 v251, v251, v85, v86\n"
    "v_max_f32_e32 v251, v251, v87\n"
    "v_mov_b32_e32 v252, v251\n"
    "s_nop 1\n"
    "v_permlane16_swap_b32_e32 v252, v251\n"
    "s_nop 0\n"
    "v_max_f32_e32 v251, v251, v252\n"
    "v_mov_b32_e32 v252, v251\n"
    "s_nop 1\n"
    "v_permlane32_swap_b32_e32 v252, v251\n"
    "s_nop 0\n"
    "v_max_f32_e32 v251, v251, v252\n"
    "v_max_f32_e32 v251, 0, v251\n"
    "v_sub_f32_e32 v252, 0, v251\n"
    "v_exp_f32_e32 v252, v252\n"
    "s_nop 0\n"
    "v_mul_f32_e32 v237, v252, v237\n"
    "v_mbcnt_lo_u32_b32 v253, -1, 0\n"
    "v_mbcnt_hi_u32_b32 v253, -1, v253\n"
    "v_and_b32_e32 v253, 48, v253\n"
    "ds_bpermute_b32 v228, v253, v252 offset:0\n"
    "ds_bpermute_b32 v229, v253, v252 offset:4\n"
    "ds_bpermute_b32 v230, v253, v252 offset:8\n"
    "ds_bpermute_b32 v231, v253, v252 offset:12\n"
    "v_sub_f32_e32 v216, v216, v251\n"
    "v_sub_f32_e32 v217, v217, v251\n"
    "v_sub_f32_e32 v218, v218, v251\n"
    "v_sub_f32_e32 v219, v219, v251\n"
    "v_sub_f32_e32 v48, v48, v251\n"
    "v_exp_f32_e32 v48, v48\n"
    "v_sub_f32_e32 v0, v0, v251\n"
    "v_sub_f32_e32 v49, v49, v251\n"
    "v_exp_f32_e32 v49, v49\n"
    "v_sub_f32_e32 v1, v1, v251\n"
    "v_sub_f32_e32 v50, v50, v251\n"
    "v_exp_f32_e32 v50, v50\n"
    "v_sub_f32_e32 v2, v2, v251\n"
    "v_sub_f32_e32 v51, v51, v251\n"
    "v_exp_f32_e32 v51, v51\n"
    "v_sub_f32_e32 v3, v3, v251\n"
    "v_sub_f32_e32 v60, v60, v251\n"
    "v_exp_f32_e32 v60, v60\n"
    "v_sub_f32_e32 v12, v12, v251\n"
    "v_sub_f32_e32 v61, v61, v251\n"
    "v_exp_f32_e32 v61, v61\n"
    "v_sub_f32_e32 v13, v13, v251\n"
    "v_sub_f32_e32 v62, v62, v251\n"
    "v_exp_f32_e32 v62, v62\n"
    "v_sub_f32_e32 v14, v14, v251\n"
    "v_sub_f32_e32 v63, v63, v251\n"
    "v_exp_f32_e32 v63, v63\n"
    "v_sub_f32_e32 v15, v15, v251\n"
    "v_sub_f32_e32 v72, v72, v251\n"
    "v_exp_f32_e32 v72, v72\n"
    "v_sub_f32_e32 v24, v24, v251\n"
    "v_sub_f32_e32 v73, v73, v251\n"
    "v_exp_f32_e32 v73, v73\n"
    "v_sub_f32_e32 v25, v25, v251\n"
    "v_sub_f32_e32 v74, v74, v251\n"
    "v_exp_f32_e32 v74, v74\n"
    "v_sub_f32_e32 v26, v26, v251\n"
    "v_sub_f32_e32 v75, v75, v251\n"
    "v_exp_f32_e32 v75, v75\n"
    "v_sub_f32_e32 v27, v27, v251\n"
    "v_sub_f32_e32 v84, v84, v251\n"
    "v_exp_f32_e32 v84, v84\n"
    "v_sub_f32_e32 v36, v36, v251\n"
    "v_sub_f32_e32 v85, v85, v251\n"
    "v_exp_f32_e32 v85, v85\n"
    "v_sub_f32_e32 v37, v37, v251\n"
    "v_sub_f32_e32 v86, v86, v251\n"
    "v_exp_f32_e32 v86, v86\n"
    "v_sub_f32_e32 v38, v38, v251\n"
    "v_sub_f32_e32 v87, v87, v251\n"
    "v_exp_f32_e32 v87, v87\n"
    "v_sub_f32_e32 v39, v39, v251\n"
    "s_nop 0\n"
    "v_add_f32_e32 v234, v48, v49\n"
    "v_add_f32_e32 v234, v50, v234\n"
    "v_add_f32_e32 v234, v51, v234\n"
    "v_add_f32_e32 v234, v60, v234\n"
    "v_add_f32_e32 v234, v61, v234\n"
    "v_add_f32_e32 v234, v62, v234\n"
    "v_add_f32_e32 v234, v63, v234\n"
    "v_add_f32_e32 v234, v72, v234\n"
    "v_add_f32_e32 v234, v73, v234\n"
    "v_add_f32_e32 v234, v74, v234\n"
    "v_add_f32_e32 v234, v75, v234\n"
    "v_add_f32_e32 v234, v84, v234\n"
    "v_add_f32_e32 v234, v85, v234\n"
    "v_add_f32_e32 v234, v86, v234\n"
    "v_add_f32_e32 v234, v87, v234\n"
    "v_cvt_pk_bf16_f32 v96, v48, v49\n"
    "v_cvt_pk_bf16_f32 v97, v50, v51\n"
    "v_cvt_pk_bf16_f32 v98, v60, v61\n"
    "v_cvt_pk_bf16_f32 v99, v62, v63\n"
    "v_cvt_pk_bf16_f32 v100, v72, v73\n"
    "v_cvt_pk_bf16_f32 v101, v74, v75\n"
    "v_cvt_pk_bf16_f32 v102, v84, v85\n"
    "v_cvt_pk_bf16_f32 v103, v86, v87\n"
    "s_waitcnt lgkmcnt(0)\n"
    "v_accvgpr_read_b32 v232, a0\n"
    "v_mul_f32_e32 v232, v228, v232\n"
    "v_accvgpr_write_b32 a0, v232\n"
    "v_accvgpr_read_b32 v233, a1\n"
    "v_mul_f32_e32 v233, v229, v233\n"
    "v_accvgpr_write_b32 a1, v233\n"
    "v_accvgpr_read_b32 v232, a2\n"
    "v_mul_f32_e32 v232, v230, v232\n"
    "v_accvgpr_write_b32 a2, v232\n"
    "v_accvgpr_read_b32 v233, a3\n"
    "v_mul_f32_e32 v233, v231, v233\n"
    "v_accvgpr_write_b32 a3, v233\n"
    "v_accvgpr_read_b32 v232, a12\n"
    "v_mul_f32_e32 v232, v228, v232\n"
    "v_accvgpr_write_b32 a12, v232\n"
    "v_accvgpr_read_b32 v233, a13\n"
    "v_mul_f32_e32 v233, v229, v233\n"
    "v_accvgpr_write_b32 a13, v233\n"
    "v_accvgpr_read_b32 v232, a14\n"
    "v_mul_f32_e32 v232, v230, v232\n"
    "v_accvgpr_write_b32 a14, v232\n"
    "v_accvgpr_read_b32 v233, a15\n"
    "v_mul_f32_e32 v233, v231, v233\n"
    "v_accvgpr_write_b32 a15, v233\n"
    "v_accvgpr_read_b32 v232, a24\n"
    "v_mul_f32_e32 v232, v228, v232\n"
    "v_accvgpr_write_b32 a24, v232\n"
    "v_accvgpr_read_b32 v233, a25\n"
    "v_mul_f32_e32 v233, v229, v233\n"
    "v_accvgpr_write_b32 a25, v233\n"
    "v_accvgpr_read_b32 v232, a26\n"
    "v_mul_f32_e32 v232, v230, v232\n"
    "v_accvgpr_write_b32 a26, v232\n"
    "v_accvgpr_read_b32 v233, a27\n"
    "v_mul_f32_e32 v233, v231, v233\n"
    "v_accvgpr_write_b32 a27, v233\n"
    "v_accvgpr_read_b32 v232, a36\n"
    "v_mul_f32_e32 v232, v228, v232\n"
    "v_accvgpr_write_b32 a36, v232\n"
    "v_accvgpr_read_b32 v233, a37\n"
    "v_mul_f32_e32 v233, v229, v233\n"
    "v_accvgpr_write_b32 a37, v233\n"
    "v_accvgpr_read_b32 v232, a38\n"
    "v_mul_f32_e32 v232, v230, v232\n"
    "v_accvgpr_write_b32 a38, v232\n"
    "v_accvgpr_read_b32 v233, a39\n"
    "v_mul_f32_e32 v233, v231, v233\n"
    "v_accvgpr_write_b32 a39, v233\n"
    "v_accvgpr_read_b32 v232, a48\n"
    "v_mul_f32_e32 v232, v228, v232\n"
    "v_accvgpr_write_b32 a48, v232\n"
    "v_accvgpr_read_b32 v233, a49\n"
    "v_mul_f32_e32 v233, v229, v233\n"
    "v_accvgpr_write_b32 a49, v233\n"
    "v_accvgpr_read_b32 v232, a50\n"
    "v_mul_f32_e32 v232, v230, v232\n"
    "v_accvgpr_write_b32 a50, v232\n"
    "v_accvgpr_read_b32 v233, a51\n"
    "v_mul_f32_e32 v233, v231, v233\n"
    "v_accvgpr_write_b32 a51, v233\n"
    "v_accvgpr_read_b32 v232, a60\n"
    "v_mul_f32_e32 v232, v228, v232\n"
    "v_accvgpr_write_b32 a60, v232\n"
    "v_accvgpr_read_b32 v233, a61\n"
    "v_mul_f32_e32 v233, v229, v233\n"
    "v_accvgpr_write_b32 a61, v233\n"
    "v_accvgpr_read_b32 v232, a62\n"
    "v_mul_f32_e32 v232, v230, v232\n"
    "v_accvgpr_write_b32 a62, v232\n"
    "v_accvgpr_read_b32 v233, a63\n"
    "v_mul_f32_e32 v233, v231, v233\n"
    "v_accvgpr_write_b32 a63, v233\n"
    "v_accvgpr_read_b32 v232, a72\n"
    "v_mul_f32_e32 v232, v228, v232\n"
    "v_accvgpr_write_b32 a72, v232\n"
    "v_accvgpr_read_b32 v233, a73\n"
    "v_mul_f32_e32 v233, v229, v233\n"
    "v_accvgpr_write_b32 a73, v233\n"
    "v_accvgpr_read_b32 v232, a74\n"
    "v_mul_f32_e32 v232, v230, v232\n"
    "v_accvgpr_write_b32 a74, v232\n"
    "v_accvgpr_read_b32 v233, a75\n"
    "v_mul_f32_e32 v233, v231, v233\n"
    "v_accvgpr_write_b32 a75, v233\n"
    "v_accvgpr_read_b32 v232, a84\n"
    "v_mul_f32_e32 v232, v228, v232\n"
    "v_accvgpr_write_b32 a84, v232\n"
    "v_accvgpr_read_b32 v233, a85\n"
    "v_mul_f32_e32 v233, v229, v233\n"
    "v_accvgpr_write_b32 a85, v233\n"
    "v_accvgpr_read_b32 v232, a86\n"
    "v_mul_f32_e32 v232, v230, v232\n"
    "v_accvgpr_write_b32 a86, v232\n"
    "v_accvgpr_read_b32 v233, a87\n"
    "v_mul_f32_e32 v233, v231, v233\n"
    "v_accvgpr_write_b32 a87, v233\n"
    "v_accvgpr_read_b32 v232, a96\n"
    "v_mul_f32_e32 v232, v228, v232\n"
    "v_accvgpr_write_b32 a96, v232\n"
    "v_accvgpr_read_b32 v233, a97\n"
    "v_mul_f32_e32 v233, v229, v233\n"
    "v_accvgpr_write_b32 a97, v233\n"
    "v_accvgpr_read_b32 v232, a98\n"
    "v_mul_f32_e32 v232, v230, v232\n"
    "v_accvgpr_write_b32 a98, v232\n"
    "v_accvgpr_read_b32 v233, a99\n"
    "v_mul_f32_e32 v233, v231, v233\n"
    "v_accvgpr_write_b32 a99, v233\n"
    "v_accvgpr_read_b32 v232, a108\n"
    "v_mul_f32_e32 v232, v228, v232\n"
    "v_accvgpr_write_b32 a108, v232\n"
    "v_accvgpr_read_b32 v233, a109\n"
    "v_mul_f32_e32 v233, v229, v233\n"
    "v_accvgpr_write_b32 a109, v233\n"
    "v_accvgpr_read_b32 v232, a110\n"
    "v_mul_f32_e32 v232, v230, v232\n"
    "v_accvgpr_write_b32 a110, v232\n"
    "v_accvgpr_read_b32 v233, a111\n"
    "v_mul_f32_e32 v233, v231, v233\n"
    "v_accvgpr_write_b32 a111, v233\n"
    "v_accvgpr_read_b32 v232, a120\n"
    "v_mul_f32_e32 v232, v228, v232\n"
    "v_accvgpr_write_b32 a120, v232\n"
    "v_accvgpr_read_b32 v233, a121\n"
    "v_mul_f32_e32 v233, v229, v233\n"
    "v_accvgpr_write_b32 a121, v233\n"
    "v_accvgpr_read_b32 v232, a122\n"
    "v_mul_f32_e32 v232, v230, v232\n"
    "v_accvgpr_write_b32 a122, v232\n"
    "v_accvgpr_read_b32 v233, a123\n"
    "v_mul_f32_e32 v233, v231, v233\n"
    "v_accvgpr_write_b32 a123, v233\n"
    "v_accvgpr_read_b32 v232, a132\n"
    "v_mul_f32_e32 v232, v228, v232\n"
    "v_accvgpr_write_b32 a132, v232\n"
    "v_accvgpr_read_b32 v233, a133\n"
    "v_mul_f32_e32 v233, v229, v233\n"
    "v_accvgpr_write_b32 a133, v233\n"
    "v_accvgpr_read_b32 v232, a134\n"
    "v_mul_f32_e32 v232, v230, v232\n"
    "v_accvgpr_write_b32 a134, v232\n"
    "v_accvgpr_read_b32 v233, a135\n"
    "v_mul_f32_e32 v233, v231, v233\n"
    "v_accvgpr_write_b32 a135, v233\n"
    "v_accvgpr_read_b32 v232, a144\n"
    "v_mul_f32_e32 v232, v228, v232\n"
    "v_accvgpr_write_b32 a144, v232\n"
    "v_accvgpr_read_b32 v233, a145\n"
    "v_mul_f32_e32 v233, v229, v233\n"
    "v_accvgpr_write_b32 a145, v233\n"
    "v_accvgpr_read_b32 v232, a146\n"
    "v_mul_f32_e32 v232, v230, v232\n"
    "v_accvgpr_write_b32 a146, v232\n"
    "v_accvgpr_read_b32 v233, a147\n"
    "v_mul_f32_e32 v233, v231, v233\n"
    "v_accvgpr_write_b32 a147, v233\n"
    "v_accvgpr_read_b32 v232, a156\n"
    "v_mul_f32_e32 v232, v228, v232\n"
    "v_accvgpr_write_b32 a156, v232\n"
    "v_accvgpr_read_b32 v233, a157\n"
    "v_mul_f32_e32 v233, v229, v233\n"
    "v_accvgpr_write_b32 a157, v233\n"
    "v_accvgpr_read_b32 v232, a158\n"
    "v_mul_f32_e32 v232, v230, v232\n"
    "v_accvgpr_write_b32 a158, v232\n"
    "v_accvgpr_read_b32 v233, a159\n"
    "v_mul_f32_e32 v233, v231, v233\n"
    "v_accvgpr_write_b32 a159, v233\n"
    "v_accvgpr_read_b32 v232, a168\n"
    "v_mul_f32_e32 v232, v228, v232\n"
    "v_accvgpr_write_b32 a168, v232\n"
    "v_accvgpr_read_b32 v233, a169\n"
    "v_mul_f32_e32 v233, v229, v233\n"
    "v_accvgpr_write_b32 a169, v233\n"
    "v_accvgpr_read_b32 v232, a170\n"
    "v_mul_f32_e32 v232, v230, v232\n"
    "v_accvgpr_write_b32 a170, v232\n"
    "v_accvgpr_read_b32 v233, a171\n"
    "v_mul_f32_e32 v233, v231, v233\n"
    "v_accvgpr_write_b32 a171, v233\n"
    "v_accvgpr_read_b32 v232, a180\n"
    "v_mul_f32_e32 v232, v228, v232\n"
    "v_accvgpr_write_b32 a180, v232\n"
    "v_accvgpr_read_b32 v233, a181\n"
    "v_mul_f32_e32 v233, v229, v233\n"
    "v_accvgpr_write_b32 a181, v233\n"
    "v_accvgpr_read_b32 v232, a182\n"
    "v_mul_f32_e32 v232, v230, v232\n"
    "v_accvgpr_write_b32 a182, v232\n"
    "v_accvgpr_read_b32 v233, a183\n"
    "v_mul_f32_e32 v233, v231, v233\n"
    "v_accvgpr_write_b32 a183, v233\n"
    "v_max3_f32 v251, v52, v53, v54\n"
    "v_max3_f32 v251, v251, v55, v64\n"
    "v_max3_f32 v251, v251, v65, v66\n"
    "v_max3_f32 v251, v251, v67, v76\n"
    "v_max3_f32 v251, v251, v77, v78\n"
    "v_max3_f32 v251, v251, v79, v88\n"
    "v_max3_f32 v251, v251, v89, v90\n"
    "v_max_f32_e32 v251, v251, v91\n"
    "v_mov_b32_e32 v252, v251\n"
    "s_nop 1\n"
    "v_permlane16_swap_b32_e32 v252, v251\n"
    "s_nop 0\n"
    "v_max_f32_e32 v251, v251, v252\n"
    "v_mov_b32_e32 v252, v251\n"
    "s_nop 1\n"
    "v_permlane32_swap_b32_e32 v252, v251\n"
    "s_nop 0\n"
    "v_max_f32_e32 v251, v251, v252\n"
    "v_max_f32_e32 v251, 0, v251\n"
    "v_sub_f32_e32 v252, 0, v251\n"
    "v_exp_f32_e32 v252, v252\n"
    "s_nop 0\n"
    "v_mul_f32_e32 v238, v252, v238\n"
    "v_mbcnt_lo_u32_b32 v253, -1, 0\n"
    "v_mbcnt_hi_u32_b32 v253, -1, v253\n"
    "v_and_b32_e32 v253, 48, v253\n"
    "ds_bpermute_b32 v228, v253, v252 offset:0\n"
    "ds_bpermute_b32 v229, v253, v252 offset:4\n"
    "ds_bpermute_b32 v230, v253, v252 offset:8\n"
    "ds_bpermute_b32 v231, v253, v252 offset:12\n"
    "v_sub_f32_e32 v220, v220, v251\n"
    "v_sub_f32_e32 v221, v221, v251\n"
    "v_sub_f32_e32 v222, v222, v251\n"
    "v_sub_f32_e32 v223, v223, v251\n"
    "v_sub_f32_e32 v52, v52, v251\n"
    "v_exp_f32_e32 v52, v52\n"
    "v_sub_f32_e32 v4, v4, v251\n"
    "v_sub_f32_e32 v53, v53, v251\n"
    "v_exp_f32_e32 v53, v53\n"
    "v_sub_f32_e32 v5, v5, v251\n"
    "v_sub_f32_e32 v54, v54, v251\n"
    "v_exp_f32_e32 v54, v54\n"
    "v_sub_f32_e32 v6, v6, v251\n"
    "v_sub_f32_e32 v55, v55, v251\n"
    "v_exp_f32_e32 v55, v55\n"
    "v_sub_f32_e32 v7, v7, v251\n"
    "v_sub_f32_e32 v64, v64, v251\n"
    "v_exp_f32_e32 v64, v64\n"
    "v_sub_f32_e32 v16, v16, v251\n"
    "v_sub_f32_e32 v65, v65, v251\n"
    "v_exp_f32_e32 v65, v65\n"
    "v_sub_f32_e32 v17, v17, v251\n"
    "v_sub_f32_e32 v66, v66, v251\n"
    "v_exp_f32_e32 v66, v66\n"
    "v_sub_f32_e32 v18, v18, v251\n"
    "v_sub_f32_e32 v67, v67, v251\n"
    "v_exp_f32_e32 v67, v67\n"
    "v_sub_f32_e32 v19, v19, v251\n"
    "v_sub_f32_e32 v76, v76, v251\n"
    "v_exp_f32_e32 v76, v76\n"
    "v_sub_f32_e32 v28, v28, v251\n"
    "v_sub_f32_e32 v77, v77, v251\n"
    "v_exp_f32_e32 v77, v77\n"
    "v_sub_f32_e32 v29, v29, v251\n"
    "v_sub_f32_e32 v78, v78, v251\n"
    "v_exp_f32_e32 v78, v78\n"
    "v_sub_f32_e32 v30, v30, v251\n"
    "v_sub_f32_e32 v79, v79, v251\n"
    "v_exp_f32_e32 v79, v79\n"
    "v_sub_f32_e32 v31, v31, v251\n"
    "v_sub_f32_e32 v88, v88, v251\n"
    "v_exp_f32_e32 v88, v88\n"
    "v_sub_f32_e32 v40, v40, v251\n"
    "v_sub_f32_e32 v89, v89, v251\n"
    "v_exp_f32_e32 v89, v89\n"
    "v_sub_f32_e32 v41, v41, v251\n"
    "v_sub_f32_e32 v90, v90, v251\n"
    "v_exp_f32_e32 v90, v90\n"
    "v_sub_f32_e32 v42, v42, v251\n"
    "v_sub_f32_e32 v91, v91, v251\n"
    "v_exp_f32_e32 v91, v91\n"
    "v_sub_f32_e32 v43, v43, v251\n"
    "s_nop 0\n"
    "v_add_f32_e32 v235, v52, v53\n"
    "v_add_f32_e32 v235, v54, v235\n"
    "v_add_f32_e32 v235, v55, v235\n"
    "v_add_f32_e32 v235, v64, v235\n"
    "v_add_f32_e32 v235, v65, v235\n"
    "v_add_f32_e32 v235, v66, v235\n"
    "v_add_f32_e32 v235, v67, v235\n"
    "v_add_f32_e32 v235, v76, v235\n"
    "v_add_f32_e32 v235, v77, v235\n"
    "v_add_f32_e32 v235, v78, v235\n"
    "v_add_f32_e32 v235, v79, v235\n"
    "v_add_f32_e32 v235, v88, v235\n"
    "v_add_f32_e32 v235, v89, v235\n"
    "v_add_f32_e32 v235, v90, v235\n"
    "v_add_f32_e32 v235, v91, v235\n"
    "v_cvt_pk_bf16_f32 v104, v52, v53\n"
    "v_cvt_pk_bf16_f32 v105, v54, v55\n"
    "v_cvt_pk_bf16_f32 v106, v64, v65\n"
    "v_cvt_pk_bf16_f32 v107, v66, v67\n"
    "v_cvt_pk_bf16_f32 v108, v76, v77\n"
    "v_cvt_pk_bf16_f32 v109, v78, v79\n"
    "v_cvt_pk_bf16_f32 v110, v88, v89\n"
    "v_cvt_pk_bf16_f32 v111, v90, v91\n"
    "s_waitcnt lgkmcnt(0)\n"
    "v_accvgpr_read_b32 v232, a4\n"
    "v_mul_f32_e32 v232, v228, v232\n"
    "v_accvgpr_write_b32 a4, v232\n"
    "v_accvgpr_read_b32 v233, a5\n"
    "v_mul_f32_e32 v233, v229, v233\n"
    "v_accvgpr_write_b32 a5, v233\n"
    "v_accvgpr_read_b32 v232, a6\n"
    "v_mul_f32_e32 v232, v230, v232\n"
    "v_accvgpr_write_b32 a6, v232\n"
    "v_accvgpr_read_b32 v233, a7\n"
    "v_mul_f32_e32 v233, v231, v233\n"
    "v_accvgpr_write_b32 a7, v233\n"
    "v_accvgpr_read_b32 v232, a16\n"
    "v_mul_f32_e32 v232, v228, v232\n"
    "v_accvgpr_write_b32 a16, v232\n"
    "v_accvgpr_read_b32 v233, a17\n"
    "v_mul_f32_e32 v233, v229, v233\n"
    "v_accvgpr_write_b32 a17, v233\n"
    "v_accvgpr_read_b32 v232, a18\n"
    "v_mul_f32_e32 v232, v230, v232\n"
    "v_accvgpr_write_b32 a18, v232\n"
    "v_accvgpr_read_b32 v233, a19\n"
    "v_mul_f32_e32 v233, v231, v233\n"
    "v_accvgpr_write_b32 a19, v233\n"
    "v_accvgpr_read_b32 v232, a28\n"
    "v_mul_f32_e32 v232, v228, v232\n"
    "v_accvgpr_write_b32 a28, v232\n"
    "v_accvgpr_read_b32 v233, a29\n"
    "v_mul_f32_e32 v233, v229, v233\n"
    "v_accvgpr_write_b32 a29, v233\n"
    "v_accvgpr_read_b32 v232, a30\n"
    "v_mul_f32_e32 v232, v230, v232\n"
    "v_accvgpr_write_b32 a30, v232\n"
    "v_accvgpr_read_b32 v233, a31\n"
    "v_mul_f32_e32 v233, v231, v233\n"
    "v_accvgpr_write_b32 a31, v233\n"
    "v_accvgpr_read_b32 v232, a40\n"
    "v_mul_f32_e32 v232, v228, v232\n"
    "v_accvgpr_write_b32 a40, v232\n"
    "v_accvgpr_read_b32 v233, a41\n"
    "v_mul_f32_e32 v233, v229, v233\n"
    "v_accvgpr_write_b32 a41, v233\n"
    "v_accvgpr_read_b32 v232, a42\n"
    "v_mul_f32_e32 v232, v230, v232\n"
    "v_accvgpr_write_b32 a42, v232\n"
    "v_accvgpr_read_b32 v233, a43\n"
    "v_mul_f32_e32 v233, v231, v233\n"
    "v_accvgpr_write_b32 a43, v233\n"
    "v_accvgpr_read_b32 v232, a52\n"
    "v_mul_f32_e32 v232, v228, v232\n"
    "v_accvgpr_write_b32 a52, v232\n"
    "v_accvgpr_read_b32 v233, a53\n"
    "v_mul_f32_e32 v233, v229, v233\n"
    "v_accvgpr_write_b32 a53, v233\n"
    "v_accvgpr_read_b32 v232, a54\n"
    "v_mul_f32_e32 v232, v230, v232\n"
    "v_accvgpr_write_b32 a54, v232\n"
    "v_accvgpr_read_b32 v233, a55\n"
    "v_mul_f32_e32 v233, v231, v233\n"
    "v_accvgpr_write_b32 a55, v233\n"
    "v_accvgpr_read_b32 v232, a64\n"
    "v_mul_f32_e32 v232, v228, v232\n"
    "v_accvgpr_write_b32 a64, v232\n"
    "v_accvgpr_read_b32 v233, a65\n"
    "v_mul_f32_e32 v233, v229, v233\n"
    "v_accvgpr_write_b32 a65, v233\n"
    "v_accvgpr_read_b32 v232, a66\n"
    "v_mul_f32_e32 v232, v230, v232\n"
    "v_accvgpr_write_b32 a66, v232\n"
    "v_accvgpr_read_b32 v233, a67\n"
    "v_mul_f32_e32 v233, v231, v233\n"
    "v_accvgpr_write_b32 a67, v233\n"
    "v_accvgpr_read_b32 v232, a76\n"
    "v_mul_f32_e32 v232, v228, v232\n"
    "v_accvgpr_write_b32 a76, v232\n"
    "v_accvgpr_read_b32 v233, a77\n"
    "v_mul_f32_e32 v233, v229, v233\n"
    "v_accvgpr_write_b32 a77, v233\n"
    "v_accvgpr_read_b32 v232, a78\n"
    "v_mul_f32_e32 v232, v230, v232\n"
    "v_accvgpr_write_b32 a78, v232\n"
    "v_accvgpr_read_b32 v233, a79\n"
    "v_mul_f32_e32 v233, v231, v233\n"
    "v_accvgpr_write_b32 a79, v233\n"
    "v_accvgpr_read_b32 v232, a88\n"
    "v_mul_f32_e32 v232, v228, v232\n"
    "v_accvgpr_write_b32 a88, v232\n"
    "v_accvgpr_read_b32 v233, a89\n"
    "v_mul_f32_e32 v233, v229, v233\n"
    "v_accvgpr_write_b32 a89, v233\n"
    "v_accvgpr_read_b32 v232, a90\n"
    "v_mul_f32_e32 v232, v230, v232\n"
    "v_accvgpr_write_b32 a90, v232\n"
    "v_accvgpr_read_b32 v233, a91\n"
    "v_mul_f32_e32 v233, v231, v233\n"
    "v_accvgpr_write_b32 a91, v233\n"
    "v_accvgpr_read_b32 v232, a100\n"
    "v_mul_f32_e32 v232, v228, v232\n"
    "v_accvgpr_write_b32 a100, v232\n"
    "v_accvgpr_read_b32 v233, a101\n"
    "v_mul_f32_e32 v233, v229, v233\n"
    "v_accvgpr_write_b32 a101, v233\n"
    "v_accvgpr_read_b32 v232, a102\n"
    "v_mul_f32_e32 v232, v230, v232\n"
    "v_accvgpr_write_b32 a102, v232\n"
    "v_accvgpr_read_b32 v233, a103\n"
    "v_mul_f32_e32 v233, v231, v233\n"
    "v_accvgpr_write_b32 a103, v233\n"
    "v_accvgpr_read_b32 v232, a112\n"
    "v_mul_f32_e32 v232, v228, v232\n"
    "v_accvgpr_write_b32 a112, v232\n"
    "v_accvgpr_read_b32 v233, a113\n"
    "v_mul_f32_e32 v233, v229, v233\n"
    "v_accvgpr_write_b32 a113, v233\n"
    "v_accvgpr_read_b32 v232, a114\n"
    "v_mul_f32_e32 v232, v230, v232\n"
    "v_accvgpr_write_b32 a114, v232\n"
    "v_accvgpr_read_b32 v233, a115\n"
    "v_mul_f32_e32 v233, v231, v233\n"
    "v_accvgpr_write_b32 a115, v233\n"
    "v_accvgpr_read_b32 v232, a124\n"
    "v_mul_f32_e32 v232, v228, v232\n"
    "v_accvgpr_write_b32 a124, v232\n"
    "v_accvgpr_read_b32 v233, a125\n"
    "v_mul_f32_e32 v233, v229, v233\n"
    "v_accvgpr_write_b32 a125, v233\n"
    "v_accvgpr_read_b32 v232, a126\n"
    "v_mul_f32_e32 v232, v230, v232\n"
    "v_accvgpr_write_b32 a126, v232\n"
    "v_accvgpr_read_b32 v233, a127\n"
    "v_mul_f32_e32 v233, v231, v233\n"
    "v_accvgpr_write_b32 a127, v233\n"
    "v_accvgpr_read_b32 v232, a136\n"
    "v_mul_f32_e32 v232, v228, v232\n"
    "v_accvgpr_write_b32 a136, v232\n"
    "v_accvgpr_read_b32 v233, a137\n"
    "v_mul_f32_e32 v233, v229, v233\n"
    "v_accvgpr_write_b32 a137, v233\n"
    "v_accvgpr_read_b32 v232, a138\n"
    "v_mul_f32_e32 v232, v230, v232\n"
    "v_accvgpr_write_b32 a138, v232\n"
    "v_accvgpr_read_b32 v233, a139\n"
    "v_mul_f32_e32 v233, v231, v233\n"
    "v_accvgpr_write_b32 a139, v233\n"
    "v_accvgpr_read_b32 v232, a148\n"
    "v_mul_f32_e32 v232, v228, v232\n"
    "v_accvgpr_write_b32 a148, v232\n"
    "v_accvgpr_read_b32 v233, a149\n"
    "v_mul_f32_e32 v233, v229, v233\n"
    "v_accvgpr_write_b32 a149, v233\n"
    "v_accvgpr_read_b32 v232, a150\n"
    "v_mul_f32_e32 v232, v230, v232\n"
    "v_accvgpr_write_b32 a150, v232\n"
    "v_accvgpr_read_b32 v233, a151\n"
    "v_mul_f32_e32 v233, v231, v233\n"
    "v_accvgpr_write_b32 a151, v233\n"
    "v_accvgpr_read_b32 v232, a160\n"
    "v_mul_f32_e32 v232, v228, v232\n"
    "v_accvgpr_write_b32 a160, v232\n"
    "v_accvgpr_read_b32 v233, a161\n"
    "v_mul_f32_e32 v233, v229, v233\n"
    "v_accvgpr_write_b32 a161, v233\n"
    "v_accvgpr_read_b32 v232, a162\n"
    "v_mul_f32_e32 v232, v230, v232\n"
    "v_accvgpr_write_b32 a162, v232\n"
    "v_accvgpr_read_b32 v233, a163\n"
    "v_mul_f32_e32 v233, v231, v233\n"
    "v_accvgpr_write_b32 a163, v233\n"
    "v_accvgpr_read_b32 v232, a172\n"
    "v_mul_f32_e32 v232, v228, v232\n"
    "v_accvgpr_write_b32 a172, v232\n"
    "v_accvgpr_read_b32 v233, a173\n"
    "v_mul_f32_e32 v233, v229, v233\n"
    "v_accvgpr_write_b32 a173, v233\n"
    "v_accvgpr_read_b32 v232, a174\n"
    "v_mul_f32_e32 v232, v230, v232\n"
    "v_accvgpr_write_b32 a174, v232\n"
    "v_accvgpr_read_b32 v233, a175\n"
    "v_mul_f32_e32 v233, v231, v233\n"
    "v_accvgpr_write_b32 a175, v233\n"
    "v_accvgpr_read_b32 v232, a184\n"
    "v_mul_f32_e32 v232, v228, v232\n"
    "v_accvgpr_write_b32 a184, v232\n"
    "v_accvgpr_read_b32 v233, a185\n"
    "v_mul_f32_e32 v233, v229, v233\n"
    "v_accvgpr_write_b32 a185, v233\n"
    "v_accvgpr_read_b32 v232, a186\n"
    "v_mul_f32_e32 v232, v230, v232\n"
    "v_accvgpr_write_b32 a186, v232\n"
    "v_accvgpr_read_b32 v233, a187\n"
    "v_mul_f32_e32 v233, v231, v233\n"
    "v_accvgpr_write_b32 a187, v233\n"
    "v_max3_f32 v251, v56, v57, v58\n"
    "v_max3_f32 v251, v251, v59, v68\n"
    "v_max3_f32 v251, v251, v69, v70\n"
    "v_max3_f32 v251, v251, v71, v80\n"
    "v_max3_f32 v251, v251, v81, v82\n"
    "v_max3_f32 v251, v251, v83, v92\n"
    "v_max3_f32 v251, v251, v93, v94\n"
    "v_max_f32_e32 v251, v251, v95\n"
    "v_mov_b32_e32 v252, v251\n"
    "s_nop 1\n"
    "v_permlane16_swap_b32_e32 v252, v251\n"
    "s_nop 0\n"
    "v_max_f32_e32 v251, v251, v252\n"
    "v_mov_b32_e32 v252, v251\n"
    "s_nop 1\n"
    "v_permlane32_swap_b32_e32 v252, v251\n"
    "s_nop 0\n"
    "v_max_f32_e32 v251, v251, v252\n"
    "v_max_f32_e32 v251, 0, v251\n"
    "v_sub_f32_e32 v252, 0, v251\n"
    "v_exp_f32_e32 v252, v252\n"
    "s_nop 0\n"
    "v_mul_f32_e32 v239, v252, v239\n"
    "v_mbcnt_lo_u32_b32 v253, -1, 0\n"
    "v_mbcnt_hi_u32_b32 v253, -1, v253\n"
    "v_and_b32_e32 v253, 48, v253\n"
    "ds_bpermute_b32 v228, v253, v252 offset:0\n"
    "ds_bpermute_b32 v229, v253, v252 offset:4\n"
    "ds_bpermute_b32 v230, v253, v252 offset:8\n"
    "ds_bpermute_b32 v231, v253, v252 offset:12\n"
    "v_sub_f32_e32 v224, v224, v251\n"
    "v_sub_f32_e32 v225, v225, v251\n"
    "v_sub_f32_e32 v226, v226, v251\n"
    "v_sub_f32_e32 v227, v227, v251\n"
    "v_sub_f32_e32 v56, v56, v251\n"
    "v_exp_f32_e32 v56, v56\n"
    "v_sub_f32_e32 v8, v8, v251\n"
    "v_sub_f32_e32 v57, v57, v251\n"
    "v_exp_f32_e32 v57, v57\n"
    "v_sub_f32_e32 v9, v9, v251\n"
    "v_sub_f32_e32 v58, v58, v251\n"
    "v_exp_f32_e32 v58, v58\n"
    "v_sub_f32_e32 v10, v10, v251\n"
    "v_sub_f32_e32 v59, v59, v251\n"
    "v_exp_f32_e32 v59, v59\n"
    "v_sub_f32_e32 v11, v11, v251\n"
    "v_sub_f32_e32 v68, v68, v251\n"
    "v_exp_f32_e32 v68, v68\n"
    "v_sub_f32_e32 v20, v20, v251\n"
    "v_sub_f32_e32 v69, v69, v251\n"
    "v_exp_f32_e32 v69, v69\n"
    "v_sub_f32_e32 v21, v21, v251\n"
    "v_sub_f32_e32 v70, v70, v251\n"
    "v_exp_f32_e32 v70, v70\n"
    "v_sub_f32_e32 v22, v22, v251\n"
    "v_sub_f32_e32 v71, v71, v251\n"
    "v_exp_f32_e32 v71, v71\n"
    "v_sub_f32_e32 v23, v23, v251\n"
    "v_sub_f32_e32 v80, v80, v251\n"
    "v_exp_f32_e32 v80, v80\n"
    "v_sub_f32_e32 v32, v32, v251\n"
    "v_sub_f32_e32 v81, v81, v251\n"
    "v_exp_f32_e32 v81, v81\n"
    "v_sub_f32_e32 v33, v33, v251\n"
    "v_sub_f32_e32 v82, v82, v251\n"
    "v_exp_f32_e32 v82, v82\n"
    "v_sub_f32_e32 v34, v34, v251\n"
    "v_sub_f32_e32 v83, v83, v251\n"
    "v_exp_f32_e32 v83, v83\n"
    "v_sub_f32_e32 v35, v35, v251\n"
    "v_sub_f32_e32 v92, v92, v251\n"
    "v_exp_f32_e32 v92, v92\n"
    "v_sub_f32_e32 v44, v44, v251\n"
    "v_sub_f32_e32 v93, v93, v251\n"
    "v_exp_f32_e32 v93, v93\n"
    "v_sub_f32_e32 v45, v45, v251\n"
    "v_sub_f32_e32 v94, v94, v251\n"
    "v_exp_f32_e32 v94, v94\n"
    "v_sub_f32_e32 v46, v46, v251\n"
    "v_sub_f32_e32 v95, v95, v251\n"
    "v_exp_f32_e32 v95, v95\n"
    "v_sub_f32_e32 v47, v47, v251\n"
    "s_nop 0\n"
    "v_add_f32_e32 v236, v56, v57\n"
    "v_add_f32_e32 v236, v58, v236\n"
    "v_add_f32_e32 v236, v59, v236\n"
    "v_add_f32_e32 v236, v68, v236\n"
    "v_add_f32_e32 v236, v69, v236\n"
    "v_add_f32_e32 v236, v70, v236\n"
    "v_add_f32_e32 v236, v71, v236\n"
    "v_add_f32_e32 v236, v80, v236\n"
    "v_add_f32_e32 v236, v81, v236\n"
    "v_add_f32_e32 v236, v82, v236\n"
    "v_add_f32_e32 v236, v83, v236\n"
    "v_add_f32_e32 v236, v92, v236\n"
    "v_add_f32_e32 v236, v93, v236\n"
    "v_add_f32_e32 v236, v94, v236\n"
    "v_add_f32_e32 v236, v95, v236\n"
    "v_cvt_pk_bf16_f32 v112, v56, v57\n"
    "v_cvt_pk_bf16_f32 v113, v58, v59\n"
    "v_cvt_pk_bf16_f32 v114, v68, v69\n"
    "v_cvt_pk_bf16_f32 v115, v70, v71\n"
    "v_cvt_pk_bf16_f32 v116, v80, v81\n"
    "v_cvt_pk_bf16_f32 v117, v82, v83\n"
    "v_cvt_pk_bf16_f32 v118, v92, v93\n"
    "v_cvt_pk_bf16_f32 v119, v94, v95\n"
    "s_waitcnt lgkmcnt(0)\n"
    "v_accvgpr_read_b32 v232, a8\n"
    "v_mul_f32_e32 v232, v228, v232\n"
    "v_accvgpr_write_b32 a8, v232\n"
    "v_accvgpr_read_b32 v233, a9\n"
    "v_mul_f32_e32 v233, v229, v233\n"
    "v_accvgpr_write_b32 a9, v233\n"
    "v_accvgpr_read_b32 v232, a10\n"
    "v_mul_f32_e32 v232, v230, v232\n"
    "v_accvgpr_write_b32 a10, v232\n"
    "v_accvgpr_read_b32 v233, a11\n"
    "v_mul_f32_e32 v233, v231, v233\n"
    "v_accvgpr_write_b32 a11, v233\n"
    "v_accvgpr_read_b32 v232, a20\n"
    "v_mul_f32_e32 v232, v228, v232\n"
    "v_accvgpr_write_b32 a20, v232\n"
    "v_accvgpr_read_b32 v233, a21\n"
    "v_mul_f32_e32 v233, v229, v233\n"
    "v_accvgpr_write_b32 a21, v233\n"
    "v_accvgpr_read_b32 v232, a22\n"
    "v_mul_f32_e32 v232, v230, v232\n"
    "v_accvgpr_write_b32 a22, v232\n"
    "v_accvgpr_read_b32 v233, a23\n"
    "v_mul_f32_e32 v233, v231, v233\n"
    "v_accvgpr_write_b32 a23, v233\n"
    "v_accvgpr_read_b32 v232, a32\n"
    "v_mul_f32_e32 v232, v228, v232\n"
    "v_accvgpr_write_b32 a32, v232\n"
    "v_accvgpr_read_b32 v233, a33\n"
    "v_mul_f32_e32 v233, v229, v233\n"
    "v_accvgpr_write_b32 a33, v233\n"
    "v_accvgpr_read_b32 v232, a34\n"
    "v_mul_f32_e32 v232, v230, v232\n"
    "v_accvgpr_write_b32 a34, v232\n"
    "v_accvgpr_read_b32 v233, a35\n"
    "v_mul_f32_e32 v233, v231, v233\n"
    "v_accvgpr_write_b32 a35, v233\n"
    "v_accvgpr_read_b32 v232, a44\n"
    "v_mul_f32_e32 v232, v228, v232\n"
    "v_accvgpr_write_b32 a44, v232\n"
    "v_accvgpr_read_b32 v233, a45\n"
    "v_mul_f32_e32 v233, v229, v233\n"
    "v_accvgpr_write_b32 a45, v233\n"
    "v_accvgpr_read_b32 v232, a46\n"
    "v_mul_f32_e32 v232, v230, v232\n"
    "v_accvgpr_write_b32 a46, v232\n"
    "v_accvgpr_read_b32 v233, a47\n"
    "v_mul_f32_e32 v233, v231, v233\n"
    "v_accvgpr_write_b32 a47, v233\n"
    "v_accvgpr_read_b32 v232, a56\n"
    "v_mul_f32_e32 v232, v228, v232\n"
    "v_accvgpr_write_b32 a56, v232\n"
    "v_accvgpr_read_b32 v233, a57\n"
    "v_mul_f32_e32 v233, v229, v233\n"
    "v_accvgpr_write_b32 a57, v233\n"
    "v_accvgpr_read_b32 v232, a58\n"
    "v_mul_f32_e32 v232, v230, v232\n"
    "v_accvgpr_write_b32 a58, v232\n"
    "v_accvgpr_read_b32 v233, a59\n"
    "v_mul_f32_e32 v233, v231, v233\n"
    "v_accvgpr_write_b32 a59, v233\n"
    "v_accvgpr_read_b32 v232, a68\n"
    "v_mul_f32_e32 v232, v228, v232\n"
    "v_accvgpr_write_b32 a68, v232\n"
    "v_accvgpr_read_b32 v233, a69\n"
    "v_mul_f32_e32 v233, v229, v233\n"
    "v_accvgpr_write_b32 a69, v233\n"
    "v_accvgpr_read_b32 v232, a70\n"
    "v_mul_f32_e32 v232, v230, v232\n"
    "v_accvgpr_write_b32 a70, v232\n"
    "v_accvgpr_read_b32 v233, a71\n"
    "v_mul_f32_e32 v233, v231, v233\n"
    "v_accvgpr_write_b32 a71, v233\n"
    "v_accvgpr_read_b32 v232, a80\n"
    "v_mul_f32_e32 v232, v228, v232\n"
    "v_accvgpr_write_b32 a80, v232\n"
    "v_accvgpr_read_b32 v233, a81\n"
    "v_mul_f32_e32 v233, v229, v233\n"
    "v_accvgpr_write_b32 a81, v233\n"
    "v_accvgpr_read_b32 v232, a82\n"
    "v_mul_f32_e32 v232, v230, v232\n"
    "v_accvgpr_write_b32 a82, v232\n"
    "v_accvgpr_read_b32 v233, a83\n"
    "v_mul_f32_e32 v233, v231, v233\n"
    "v_accvgpr_write_b32 a83, v233\n"
    "v_accvgpr_read_b32 v232, a92\n"
    "v_mul_f32_e32 v232, v228, v232\n"
    "v_accvgpr_write_b32 a92, v232\n"
    "v_accvgpr_read_b32 v233, a93\n"
    "v_mul_f32_e32 v233, v229, v233\n"
    "v_accvgpr_write_b32 a93, v233\n"
    "v_accvgpr_read_b32 v232, a94\n"
    "v_mul_f32_e32 v232, v230, v232\n"
    "v_accvgpr_write_b32 a94, v232\n"
    "v_accvgpr_read_b32 v233, a95\n"
    "v_mul_f32_e32 v233, v231, v233\n"
    "v_accvgpr_write_b32 a95, v233\n"
    "v_accvgpr_read_b32 v232, a104\n"
    "v_mul_f32_e32 v232, v228, v232\n"
    "v_accvgpr_write_b32 a104, v232\n"
    "v_accvgpr_read_b32 v233, a105\n"
    "v_mul_f32_e32 v233, v229, v233\n"
    "v_accvgpr_write_b32 a105, v233\n"
    "v_accvgpr_read_b32 v232, a106\n"
    "v_mul_f32_e32 v232, v230, v232\n"
    "v_accvgpr_write_b32 a106, v232\n"
    "v_accvgpr_read_b32 v233, a107\n"
    "v_mul_f32_e32 v233, v231, v233\n"
    "v_accvgpr_write_b32 a107, v233\n"
    "v_accvgpr_read_b32 v232, a116\n"
    "v_mul_f32_e32 v232, v228, v232\n"
    "v_accvgpr_write_b32 a116, v232\n"
    "v_accvgpr_read_b32 v233, a117\n"
    "v_mul_f32_e32 v233, v229, v233\n"
    "v_accvgpr_write_b32 a117, v233\n"
    "v_accvgpr_read_b32 v232, a118\n"
    "v_mul_f32_e32 v232, v230, v232\n"
    "v_accvgpr_write_b32 a118, v232\n"
    "v_accvgpr_read_b32 v233, a119\n"
    "v_mul_f32_e32 v233, v231, v233\n"
    "v_accvgpr_write_b32 a119, v233\n"
    "v_accvgpr_read_b32 v232, a128\n"
    "v_mul_f32_e32 v232, v228, v232\n"
    "v_accvgpr_write_b32 a128, v232\n"
    "v_accvgpr_read_b32 v233, a129\n"
    "v_mul_f32_e32 v233, v229, v233\n"
    "v_accvgpr_write_b32 a129, v233\n"
    "v_accvgpr_read_b32 v232, a130\n"
    "v_mul_f32_e32 v232, v230, v232\n"
    "v_accvgpr_write_b32 a130, v232\n"
    "v_accvgpr_read_b32 v233, a131\n"
    "v_mul_f32_e32 v233, v231, v233\n"
    "v_accvgpr_write_b32 a131, v233\n"
    "v_accvgpr_read_b32 v232, a140\n"
    "v_mul_f32_e32 v232, v228, v232\n"
    "v_accvgpr_write_b32 a140, v232\n"
    "v_accvgpr_read_b32 v233, a141\n"
    "v_mul_f32_e32 v233, v229, v233\n"
    "v_accvgpr_write_b32 a141, v233\n"
    "v_accvgpr_read_b32 v232, a142\n"
    "v_mul_f32_e32 v232, v230, v232\n"
    "v_accvgpr_write_b32 a142, v232\n"
    "v_accvgpr_read_b32 v233, a143\n"
    "v_mul_f32_e32 v233, v231, v233\n"
    "v_accvgpr_write_b32 a143, v233\n"
    "v_accvgpr_read_b32 v232, a152\n"
    "v_mul_f32_e32 v232, v228, v232\n"
    "v_accvgpr_write_b32 a152, v232\n"
    "v_accvgpr_read_b32 v233, a153\n"
    "v_mul_f32_e32 v233, v229, v233\n"
    "v_accvgpr_write_b32 a153, v233\n"
    "v_accvgpr_read_b32 v232, a154\n"
    "v_mul_f32_e32 v232, v230, v232\n"
    "v_accvgpr_write_b32 a154, v232\n"
    "v_accvgpr_read_b32 v233, a155\n"
    "v_mul_f32_e32 v233, v231, v233\n"
    "v_accvgpr_write_b32 a155, v233\n"
    "v_accvgpr_read_b32 v232, a164\n"
    "v_mul_f32_e32 v232, v228, v232\n"
    "v_accvgpr_write_b32 a164, v232\n"
    "v_accvgpr_read_b32 v233, a165\n"
    "v_mul_f32_e32 v233, v229, v233\n"
    "v_accvgpr_write_b32 a165, v233\n"
    "v_accvgpr_read_b32 v232, a166\n"
    "v_mul_f32_e32 v232, v230, v232\n"
    "v_accvgpr_write_b32 a166, v232\n"
    "v_accvgpr_read_b32 v233, a167\n"
    "v_mul_f32_e32 v233, v231, v233\n"
    "v_accvgpr_write_b32 a167, v233\n"
    "v_accvgpr_read_b32 v232, a176\n"
    "v_mul_f32_e32 v232, v228, v232\n"
    "v_accvgpr_write_b32 a176, v232\n"
    "v_accvgpr_read_b32 v233, a177\n"
    "v_mul_f32_e32 v233, v229, v233\n"
    "v_accvgpr_write_b32 a177, v233\n"
    "v_accvgpr_read_b32 v232, a178\n"
    "v_mul_f32_e32 v232, v230, v232\n"
    "v_accvgpr_write_b32 a178, v232\n"
    "v_accvgpr_read_b32 v233, a179\n"
    "v_mul_f32_e32 v233, v231, v233\n"
    "v_accvgpr_write_b32 a179, v233\n"
    "v_accvgpr_read_b32 v232, a188\n"
    "v_mul_f32_e32 v232, v228, v232\n"
    "v_accvgpr_write_b32 a188, v232\n"
    "v_accvgpr_read_b32 v233, a189\n"
    "v_mul_f32_e32 v233, v229, v233\n"
    "v_accvgpr_write_b32 a189, v233\n"
    "v_accvgpr_read_b32 v232, a190\n"
    "v_mul_f32_e32 v232, v230, v232\n"
    "v_accvgpr_write_b32 a190, v232\n"
    "v_accvgpr_read_b32 v233, a191\n"
    "v_mul_f32_e32 v233, v231, v233\n"
    "v_accvgpr_write_b32 a191, v233\n"
    "s_nop 1\n"
    "s_branch RET0_%=\n"
    "RARE1_%=:\n"
    "s_nop 7\n"
    "s_waitcnt lgkmcnt(0)\n"
    "v_max3_f32 v251, v0, v1, v2\n"
    "v_max3_f32 v251, v251, v3, v12\n"
    "v_max3_f32 v251, v251, v13, v14\n"
    "v_max3_f32 v251, v251, v15, v24\n"
    "v_max3_f32 v251, v251, v25, v26\n"
    "v_max3_f32 v251, v251, v27, v36\n"
    "v_max3_f32 v251, v251, v37, v38\n"
    "v_max_f32_e32 v251, v251, v39\n"
    "v_mov_b32_e32 v252, v251\n"
    "s_nop 1\n"
    "v_permlane16_swap_b32_e32 v252, v251\n"
    "s_nop 0\n"
    "v_max_f32_e32 v251, v251, v252\n"
    "v_mov_b32_e32 v252, v251\n"
    "s_nop 1\n"
    "v_permlane32_swap_b32_e32 v252, v251\n"
    "s_nop 0\n"
    "v_max_f32_e32 v251, v251, v252\n"
    "v_max_f32_e32 v251, 0, v251\n"
    "v_sub_f32_e32 v252, 0, v251\n"
    "v_exp_f32_e32 v252, v252\n"
    "s_nop 0\n"
    "v_mul_f32_e32 v237, v252, v237\n"
    "v_mbcnt_lo_u32_b32 v253, -1, 0\n"
    "v_mbcnt_hi_u32_b32 v253, -1, v253\n"
    "v_and_b32_e32 v253, 48, v253\n"
    "ds_bpermute_b32 v228, v253, v252 offset:0\n"
    "ds_bpermute_b32 v229, v253, v252 offset:4\n"
    "ds_bpermute_b32 v230, v253, v252 offset:8\n"
    "ds_bpermute_b32 v231, v253, v252 offset:12\n"
    "v_sub_f32_e32 v216, v216, v251\n"
    "v_sub_f32_e32 v217, v217, v251\n"
    "v_sub_f32_e32 v218, v218, v251\n"
    "v_sub_f32_e32 v219, v219, v251\n"
    "v_sub_f32_e32 v0, v0, v251\n"
    "v_exp_f32_e32 v0, v0\n"
    "v_sub_f32_e32 v48, v48, v251\n"
    "v_sub_f32_e32 v1, v1, v251\n"
    "v_exp_f32_e32 v1, v1\n"
    "v_sub_f32_e32 v49, v49, v251\n"
    "v_sub_f32_e32 v2, v2, v251\n"
    "v_exp_f32_e32 v2, v2\n"
    "v_sub_f32_e32 v50, v50, v251\n"
    "v_sub_f32_e32 v3, v3, v251\n"
    "v_exp_f32_e32 v3, v3\n"
    "v_sub_f32_e32 v51, v51, v251\n"
    "v_sub_f32_e32 v12, v12, v251\n"
    "v_exp_f32_e32 v12, v12\n"
    "v_sub_f32_e32 v60, v60, v251\n"
    "v_sub_f32_e32 v13, v13, v251\n"
    "v_exp_f32_e32 v13, v13\n"
    "v_sub_f32_e32 v61, v61, v251\n"
    "v_sub_f32_e32 v14, v14, v251\n"
    "v_exp_f32_e32 v14, v14\n"
    "v_sub_f32_e32 v62, v62, v251\n"
    "v_sub_f32_e32 v15, v15, v251\n"
    "v_exp_f32_e32 v15, v15\n"
    "v_sub_f32_e32 v63, v63, v251\n"
    "v_sub_f32_e32 v24, v24, v251\n"
    "v_exp_f32_e32 v24, v24\n"
    "v_sub_f32_e32 v72, v72, v251\n"
    "v_sub_f32_e32 v25, v25, v251\n"
    "v_exp_f32_e32 v25, v25\n"
    "v_sub_f32_e32 v73, v73, v251\n"
    "v_sub_f32_e32 v26, v26, v251\n"
    "v_exp_f32_e32 v26, v26\n"
    "v_sub_f32_e32 v74, v74, v251\n"
    "v_sub_f32_e32 v27, v27, v251\n"
    "v_exp_f32_e32 v27, v27\n"
    "v_sub_f32_e32 v75, v75, v251\n"
    "v_sub_f32_e32 v36, v36, v251\n"
    "v_exp_f32_e32 v36, v36\n"
    "v_sub_f32_e32 v84, v84, v251\n"
    "v_sub_f32_e32 v37, v37, v251\n"
    "v_exp_f32_e32 v37, v37\n"
    "v_sub_f32_e32 v85, v85, v251\n"
    "v_sub_f32_e32 v38, v38, v251\n"
    "v_exp_f32_e32 v38, v38\n"
    "v_sub_f32_e32 v86, v86, v251\n"
    "v_sub_f32_e32 v39, v39, v251\n"
    "v_exp_f32_e32 v39, v39\n"
    "v_sub_f32_e32 v87, v87, v251\n"
    "s_nop 0\n"
    "v_add_f32_e32 v234, v0, v1\n"
    "v_add_f32_e32 v234, v2, v234\n"
    "v_add_f32_e32 v234, v3, v234\n"
    "v_add_f32_e32 v234, v12, v234\n"
    "v_add_f32_e32 v234, v13, v234\n"
    "v_add_f32_e32 v234, v14, v234\n"
    "v_add_f32_e32 v234, v15, v234\n"
    "v_add_f32_e32 v234, v24, v234\n"
    "v_add_f32_e32 v234, v25, v234\n"
    "v_add_f32_e32 v234, v26, v234\n"
    "v_add_f32_e32 v234, v27, v234\n"
    "v_add_f32_e32 v234, v36, v234\n"
    "v_add_f32_e32 v234, v37, v234\n"
    "v_add_f32_e32 v234, v38, v234\n"
    "v_add_f32_e32 v234, v39, v234\n"
    "v_cvt_pk_bf16_f32 v96, v0, v1\n"
    "v_cvt_pk_bf16_f32 v97, v2, v3\n"
    "v_cvt_pk_bf16_f32 v98, v12, v13\n"
    "v_cvt_pk_bf16_f32 v99, v14, v15\n"
    "v_cvt_pk_bf16_f32 v100, v24, v25\n"
    "v_cvt_pk_bf16_f32 v101, v26, v27\n"
    "v_cvt_pk_bf16_f32 v102, v36, v37\n"
    "v_cvt_pk_bf16_f32 v103, v38, v39\n"
    "s_waitcnt lgkmcnt(0)\n"
    "v_accvgpr_read_b32 v232, a0\n"
    "v_mul_f32_e32 v232, v228, v232\n"
    "v_accvgpr_write_b32 a0, v232\n"
    "v_accvgpr_read_b32 v233, a1\n"
    "v_mul_f32_e32 v233, v229, v233\n"
    "v_accvgpr_write_b32 a1, v233\n"
    "v_accvgpr_read_b32 v232, a2\n"
    "v_mul_f32_e32 v232, v230, v232\n"
    "v_accvgpr_write_b32 a2, v232\n"
    "v_accvgpr_read_b32 v233, a3\n"
    "v_mul_f32_e32 v233, v231, v233\n"
    "v_accvgpr_write_b32 a3, v233\n"
    "v_accvgpr_read_b32 v232, a12\n"
    "v_mul_f32_e32 v232, v228, v232\n"
    "v_accvgpr_write_b32 a12, v232\n"
    "v_accvgpr_read_b32 v233, a13\n"
    "v_mul_f32_e32 v233, v229, v233\n"
    "v_accvgpr_write_b32 a13, v233\n"
    "v_accvgpr_read_b32 v232, a14\n"
    "v_mul_f32_e32 v232, v230, v232\n"
    "v_accvgpr_write_b32 a14, v232\n"
    "v_accvgpr_read_b32 v233, a15\n"
    "v_mul_f32_e32 v233, v231, v233\n"
    "v_accvgpr_write_b32 a15, v233\n"
    "v_accvgpr_read_b32 v232, a24\n"
    "v_mul_f32_e32 v232, v228, v232\n"
    "v_accvgpr_write_b32 a24, v232\n"
    "v_accvgpr_read_b32 v233, a25\n"
    "v_mul_f32_e32 v233, v229, v233\n"
    "v_accvgpr_write_b32 a25, v233\n"
    "v_accvgpr_read_b32 v232, a26\n"
    "v_mul_f32_e32 v232, v230, v232\n"
    "v_accvgpr_write_b32 a26, v232\n"
    "v_accvgpr_read_b32 v233, a27\n"
    "v_mul_f32_e32 v233, v231, v233\n"
    "v_accvgpr_write_b32 a27, v233\n"
    "v_accvgpr_read_b32 v232, a36\n"
    "v_mul_f32_e32 v232, v228, v232\n"
    "v_accvgpr_write_b32 a36, v232\n"
    "v_accvgpr_read_b32 v233, a37\n"
    "v_mul_f32_e32 v233, v229, v233\n"
    "v_accvgpr_write_b32 a37, v233\n"
    "v_accvgpr_read_b32 v232, a38\n"
    "v_mul_f32_e32 v232, v230, v232\n"
    "v_accvgpr_write_b32 a38, v232\n"
    "v_accvgpr_read_b32 v233, a39\n"
    "v_mul_f32_e32 v233, v231, v233\n"
    "v_accvgpr_write_b32 a39, v233\n"
    "v_accvgpr_read_b32 v232, a48\n"
    "v_mul_f32_e32 v232, v228, v232\n"
    "v_accvgpr_write_b32 a48, v232\n"
    "v_accvgpr_read_b32 v233, a49\n"
    "v_mul_f32_e32 v233, v229, v233\n"
    "v_accvgpr_write_b32 a49, v233\n"
    "v_accvgpr_read_b32 v232, a50\n"
    "v_mul_f32_e32 v232, v230, v232\n"
    "v_accvgpr_write_b32 a50, v232\n"
    "v_accvgpr_read_b32 v233, a51\n"
    "v_mul_f32_e32 v233, v231, v233\n"
    "v_accvgpr_write_b32 a51, v233\n"
    "v_accvgpr_read_b32 v232, a60\n"
    "v_mul_f32_e32 v232, v228, v232\n"
    "v_accvgpr_write_b32 a60, v232\n"
    "v_accvgpr_read_b32 v233, a61\n"
    "v_mul_f32_e32 v233, v229, v233\n"
    "v_accvgpr_write_b32 a61, v233\n"
    "v_accvgpr_read_b32 v232, a62\n"
    "v_mul_f32_e32 v232, v230, v232\n"
    "v_accvgpr_write_b32 a62, v232\n"
    "v_accvgpr_read_b32 v233, a63\n"
    "v_mul_f32_e32 v233, v231, v233\n"
    "v_accvgpr_write_b32 a63, v233\n"
    "v_accvgpr_read_b32 v232, a72\n"
    "v_mul_f32_e32 v232, v228, v232\n"
    "v_accvgpr_write_b32 a72, v232\n"
    "v_accvgpr_read_b32 v233, a73\n"
    "v_mul_f32_e32 v233, v229, v233\n"
    "v_accvgpr_write_b32 a73, v233\n"
    "v_accvgpr_read_b32 v232, a74\n"
    "v_mul_f32_e32 v232, v230, v232\n"
    "v_accvgpr_write_b32 a74, v232\n"
    "v_accvgpr_read_b32 v233, a75\n"
    "v_mul_f32_e32 v233, v231, v233\n"
    "v_accvgpr_write_b32 a75, v233\n"
    "v_accvgpr_read_b32 v232, a84\n"
    "v_mul_f32_e32 v232, v228, v232\n"
    "v_accvgpr_write_b32 a84, v232\n"
    "v_accvgpr_read_b32 v233, a85\n"
    "v_mul_f32_e32 v233, v229, v233\n"
    "v_accvgpr_write_b32 a85, v233\n"
    "v_accvgpr_read_b32 v232, a86\n"
    "v_mul_f32_e32 v232, v230, v232\n"
    "v_accvgpr_write_b32 a86, v232\n"
    "v_accvgpr_read_b32 v233, a87\n"
    "v_mul_f32_e32 v233, v231, v233\n"
    "v_accvgpr_write_b32 a87, v233\n"
    "v_accvgpr_read_b32 v232, a96\n"
    "v_mul_f32_e32 v232, v228, v232\n"
    "v_accvgpr_write_b32 a96, v232\n"
    "v_accvgpr_read_b32 v233, a97\n"
    "v_mul_f32_e32 v233, v229, v233\n"
    "v_accvgpr_write_b32 a97, v233\n"
    "v_accvgpr_read_b32 v232, a98\n"
    "v_mul_f32_e32 v232, v230, v232\n"
    "v_accvgpr_write_b32 a98, v232\n"
    "v_accvgpr_read_b32 v233, a99\n"
    "v_mul_f32_e32 v233, v231, v233\n"
    "v_accvgpr_write_b32 a99, v233\n"
    "v_accvgpr_read_b32 v232, a108\n"
    "v_mul_f32_e32 v232, v228, v232\n"
    "v_accvgpr_write_b32 a108, v232\n"
    "v_accvgpr_read_b32 v233, a109\n"
    "v_mul_f32_e32 v233, v229, v233\n"
    "v_accvgpr_write_b32 a109, v233\n"
    "v_accvgpr_read_b32 v232, a110\n"
    "v_mul_f32_e32 v232, v230, v232\n"
    "v_accvgpr_write_b32 a110, v232\n"
    "v_accvgpr_read_b32 v233, a111\n"
    "v_mul_f32_e32 v233, v231, v233\n"
    "v_accvgpr_write_b32 a111, v233\n"
    "v_accvgpr_read_b32 v232, a120\n"
    "v_mul_f32_e32 v232, v228, v232\n"
    "v_accvgpr_write_b32 a120, v232\n"
    "v_accvgpr_read_b32 v233, a121\n"
    "v_mul_f32_e32 v233, v229, v233\n"
    "v_accvgpr_write_b32 a121, v233\n"
    "v_accvgpr_read_b32 v232, a122\n"
    "v_mul_f32_e32 v232, v230, v232\n"
    "v_accvgpr_write_b32 a122, v232\n"
    "v_accvgpr_read_b32 v233, a123\n"
    "v_mul_f32_e32 v233, v231, v233\n"
    "v_accvgpr_write_b32 a123, v233\n"
    "v_accvgpr_read_b32 v232, a132\n"
    "v_mul_f32_e32 v232, v228, v232\n"
    "v_accvgpr_write_b32 a132, v232\n"
    "v_accvgpr_read_b32 v233, a133\n"
    "v_mul_f32_e32 v233, v229, v233\n"
    "v_accvgpr_write_b32 a133, v233\n"
    "v_accvgpr_read_b32 v232, a134\n"
    "v_mul_f32_e32 v232, v230, v232\n"
    "v_accvgpr_write_b32 a134, v232\n"
    "v_accvgpr_read_b32 v233, a135\n"
    "v_mul_f32_e32 v233, v231, v233\n"
    "v_accvgpr_write_b32 a135, v233\n"
    "v_accvgpr_read_b32 v232, a144\n"
    "v_mul_f32_e32 v232, v228, v232\n"
    "v_accvgpr_write_b32 a144, v232\n"
    "v_accvgpr_read_b32 v233, a145\n"
    "v_mul_f32_e32 v233, v229, v233\n"
    "v_accvgpr_write_b32 a145, v233\n"
    "v_accvgpr_read_b32 v232, a146\n"
    "v_mul_f32_e32 v232, v230, v232\n"
    "v_accvgpr_write_b32 a146, v232\n"
    "v_accvgpr_read_b32 v233, a147\n"
    "v_mul_f32_e32 v233, v231, v233\n"
    "v_accvgpr_write_b32 a147, v233\n"
    "v_accvgpr_read_b32 v232, a156\n"
    "v_mul_f32_e32 v232, v228, v232\n"
    "v_accvgpr_write_b32 a156, v232\n"
    "v_accvgpr_read_b32 v233, a157\n"
    "v_mul_f32_e32 v233, v229, v233\n"
    "v_accvgpr_write_b32 a157, v233\n"
    "v_accvgpr_read_b32 v232, a158\n"
    "v_mul_f32_e32 v232, v230, v232\n"
    "v_accvgpr_write_b32 a158, v232\n"
    "v_accvgpr_read_b32 v233, a159\n"
    "v_mul_f32_e32 v233, v231, v233\n"
    "v_accvgpr_write_b32 a159, v233\n"
    "v_accvgpr_read_b32 v232, a168\n"
    "v_mul_f32_e32 v232, v228, v232\n"
    "v_accvgpr_write_b32 a168, v232\n"
    "v_accvgpr_read_b32 v233, a169\n"
    "v_mul_f32_e32 v233, v229, v233\n"
    "v_accvgpr_write_b32 a169, v233\n"
    "v_accvgpr_read_b32 v232, a170\n"
    "v_mul_f32_e32 v232, v230, v232\n"
    "v_accvgpr_write_b32 a170, v232\n"
    "v_accvgpr_read_b32 v233, a171\n"
    "v_mul_f32_e32 v233, v231, v233\n"
    "v_accvgpr_write_b32 a171, v233\n"
    "v_accvgpr_read_b32 v232, a180\n"
    "v_mul_f32_e32 v232, v228, v232\n"
    "v_accvgpr_write_b32 a180, v232\n"
    "v_accvgpr_read_b32 v233, a181\n"
    "v_mul_f32_e32 v233, v229, v233\n"
    "v_accvgpr_write_b32 a181, v233\n"
    "v_accvgpr_read_b32 v232, a182\n"
    "v_mul_f32_e32 v232, v230, v232\n"
    "v_accvgpr_write_b32 a182, v232\n"
    "v_accvgpr_read_b32 v233, a183\n"
    "v_mul_f32_e32 v233, v231, v233\n"
    "v_accvgpr_write_b32 a183, v233\n"
    "v_max3_f32 v251, v4, v5, v6\n"
    "v_max3_f32 v251, v251, v7, v16\n"
    "v_max3_f32 v251, v251, v17, v18\n"
    "v_max3_f32 v251, v251, v19, v28\n"
    "v_max3_f32 v251, v251, v29, v30\n"
    "v_max3_f32 v251, v251, v31, v40\n"
    "v_max3_f32 v251, v251, v41, v42\n"
    "v_max_f32_e32 v251, v251, v43\n"
    "v_mov_b32_e32 v252, v251\n"
    "s_nop 1\n"
    "v_permlane16_swap_b32_e32 v252, v251\n"
    "s_nop 0\n"
    "v_max_f32_e32 v251, v251, v252\n"
    "v_mov_b32_e32 v252, v251\n"
    "s_nop 1\n"
    "v_permlane32_swap_b32_e32 v252, v251\n"
    "s_nop 0\n"
    "v_max_f32_e32 v251, v251, v252\n"
    "v_max_f32_e32 v251, 0, v251\n"
    "v_sub_f32_e32 v252, 0, v251\n"
    "v_exp_f32_e32 v252, v252\n"
    "s_nop 0\n"
    "v_mul_f32_e32 v238, v252, v238\n"
    "v_mbcnt_lo_u32_b32 v253, -1, 0\n"
    "v_mbcnt_hi_u32_b32 v253, -1, v253\n"
    "v_and_b32_e32 v253, 48, v253\n"
    "ds_bpermute_b32 v228, v253, v252 offset:0\n"
    "ds_bpermute_b32 v229, v253, v252 offset:4\n"
    "ds_bpermute_b32 v230, v253, v252 offset:8\n"
    "ds_bpermute_b32 v231, v253, v252 offset:12\n"
    "v_sub_f32_e32 v220, v220, v251\n"
    "v_sub_f32_e32 v221, v221, v251\n"
    "v_sub_f32_e32 v222, v222, v251\n"
    "v_sub_f32_e32 v223, v223, v251\n"
    "v_sub_f32_e32 v4, v4, v251\n"
    "v_exp_f32_e32 v4, v4\n"
    "v_sub_f32_e32 v52, v52, v251\n"
    "v_sub_f32_e32 v5, v5, v251\n"
    "v_exp_f32_e32 v5, v5\n"
    "v_sub_f32_e32 v53, v53, v251\n"
    "v_sub_f32_e32 v6, v6, v251\n"
    "v_exp_f32_e32 v6, v6\n"
    "v_sub_f32_e32 v54, v54, v251\n"
    "v_sub_f32_e32 v7, v7, v251\n"
    "v_exp_f32_e32 v7, v7\n"
    "v_sub_f32_e32 v55, v55, v251\n"
    "v_sub_f32_e32 v16, v16, v251\n"
    "v_exp_f32_e32 v16, v16\n"
    "v_sub_f32_e32 v64, v64, v251\n"
    "v_sub_f32_e32 v17, v17, v251\n"
    "v_exp_f32_e32 v17, v17\n"
    "v_sub_f32_e32 v65, v65, v251\n"
    "v_sub_f32_e32 v18, v18, v251\n"
    "v_exp_f32_e32 v18, v18\n"
    "v_sub_f32_e32 v66, v66, v251\n"
    "v_sub_f32_e32 v19, v19, v251\n"
    "v_exp_f32_e32 v19, v19\n"
    "v_sub_f32_e32 v67, v67, v251\n"
    "v_sub_f32_e32 v28, v28, v251\n"
    "v_exp_f32_e32 v28, v28\n"
    "v_sub_f32_e32 v76, v76, v251\n"
    "v_sub_f32_e32 v29, v29, v251\n"
    "v_exp_f32_e32 v29, v29\n"
    "v_sub_f32_e32 v77, v77, v251\n"
    "v_sub_f32_e32 v30, v30, v251\n"
    "v_exp_f32_e32 v30, v30\n"
    "v_sub_f32_e32 v78, v78, v251\n"
    "v_sub_f32_e32 v31, v31, v251\n"
    "v_exp_f32_e32 v31, v31\n"
    "v_sub_f32_e32 v79, v79, v251\n"
    "v_sub_f32_e32 v40, v40, v251\n"
    "v_exp_f32_e32 v40, v40\n"
    "v_sub_f32_e32 v88, v88, v251\n"
    "v_sub_f32_e32 v41, v41, v251\n"
    "v_exp_f32_e32 v41, v41\n"
    "v_sub_f32_e32 v89, v89, v251\n"
    "v_sub_f32_e32 v42, v42, v251\n"
    "v_exp_f32_e32 v42, v42\n"
    "v_sub_f32_e32 v90, v90, v251\n"
    "v_sub_f32_e32 v43, v43, v251\n"
    "v_exp_f32_e32 v43, v43\n"
    "v_sub_f32_e32 v91, v91, v251\n"
    "s_nop 0\n"
    "v_add_f32_e32 v235, v4, v5\n"
    "v_add_f32_e32 v235, v6, v235\n"
    "v_add_f32_e32 v235, v7, v235\n"
    "v_add_f32_e32 v235, v16, v235\n"
    "v_add_f32_e32 v235, v17, v235\n"
    "v_add_f32_e32 v235, v18, v235\n"
    "v_add_f32_e32 v235, v19, v235\n"
    "v_add_f32_e32 v235, v28, v235\n"
    "v_add_f32_e32 v235, v29, v235\n"
    "v_add_f32_e32 v235, v30, v235\n"
    "v_add_f32_e32 v235, v31, v235\n"
    "v_add_f32_e32 v235, v40, v235\n"
    "v_add_f32_e32 v235, v41, v235\n"
    "v_add_f32_e32 v235, v42, v235\n"
    "v_add_f32_e32 v235, v43, v235\n"
    "v_cvt_pk_bf16_f32 v104, v4, v5\n"
    "v_cvt_pk_bf16_f32 v105, v6, v7\n"
    "v_cvt_pk_bf16_f32 v106, v16, v17\n"
    "v_cvt_pk_bf16_f32 v107, v18, v19\n"
    "v_cvt_pk_bf16_f32 v108, v28, v29\n"
    "v_cvt_pk_bf16_f32 v109, v30, v31\n"
    "v_cvt_pk_bf16_f32 v110, v40, v41\n"
    "v_cvt_pk_bf16_f32 v111, v42, v43\n"
    "s_waitcnt lgkmcnt(0)\n"
    "v_accvgpr_read_b32 v232, a4\n"
    "v_mul_f32_e32 v232, v228, v232\n"
    "v_accvgpr_write_b32 a4, v232\n"
    "v_accvgpr_read_b32 v233, a5\n"
    "v_mul_f32_e32 v233, v229, v233\n"
    "v_accvgpr_write_b32 a5, v233\n"
    "v_accvgpr_read_b32 v232, a6\n"
    "v_mul_f32_e32 v232, v230, v232\n"
    "v_accvgpr_write_b32 a6, v232\n"
    "v_accvgpr_read_b32 v233, a7\n"
    "v_mul_f32_e32 v233, v231, v233\n"
    "v_accvgpr_write_b32 a7, v233\n"
    "v_accvgpr_read_b32 v232, a16\n"
    "v_mul_f32_e32 v232, v228, v232\n"
    "v_accvgpr_write_b32 a16, v232\n"
    "v_accvgpr_read_b32 v233, a17\n"
    "v_mul_f32_e32 v233, v229, v233\n"
    "v_accvgpr_write_b32 a17, v233\n"
    "v_accvgpr_read_b32 v232, a18\n"
    "v_mul_f32_e32 v232, v230, v232\n"
    "v_accvgpr_write_b32 a18, v232\n"
    "v_accvgpr_read_b32 v233, a19\n"
    "v_mul_f32_e32 v233, v231, v233\n"
    "v_accvgpr_write_b32 a19, v233\n"
    "v_accvgpr_read_b32 v232, a28\n"
    "v_mul_f32_e32 v232, v228, v232\n"
    "v_accvgpr_write_b32 a28, v232\n"
    "v_accvgpr_read_b32 v233, a29\n"
    "v_mul_f32_e32 v233, v229, v233\n"
    "v_accvgpr_write_b32 a29, v233\n"
    "v_accvgpr_read_b32 v232, a30\n"
    "v_mul_f32_e32 v232, v230, v232\n"
    "v_accvgpr_write_b32 a30, v232\n"
    "v_accvgpr_read_b32 v233, a31\n"
    "v_mul_f32_e32 v233, v231, v233\n"
    "v_accvgpr_write_b32 a31, v233\n"
    "v_accvgpr_read_b32 v232, a40\n"
    "v_mul_f32_e32 v232, v228, v232\n"
    "v_accvgpr_write_b32 a40, v232\n"
    "v_accvgpr_read_b32 v233, a41\n"
    "v_mul_f32_e32 v233, v229, v233\n"
    "v_accvgpr_write_b32 a41, v233\n"
    "v_accvgpr_read_b32 v232, a42\n"
    "v_mul_f32_e32 v232, v230, v232\n"
    "v_accvgpr_write_b32 a42, v232\n"
    "v_accvgpr_read_b32 v233, a43\n"
    "v_mul_f32_e32 v233, v231, v233\n"
    "v_accvgpr_write_b32 a43, v233\n"
    "v_accvgpr_read_b32 v232, a52\n"
    "v_mul_f32_e32 v232, v228, v232\n"
    "v_accvgpr_write_b32 a52, v232\n"
    "v_accvgpr_read_b32 v233, a53\n"
    "v_mul_f32_e32 v233, v229, v233\n"
    "v_accvgpr_write_b32 a53, v233\n"
    "v_accvgpr_read_b32 v232, a54\n"
    "v_mul_f32_e32 v232, v230, v232\n"
    "v_accvgpr_write_b32 a54, v232\n"
    "v_accvgpr_read_b32 v233, a55\n"
    "v_mul_f32_e32 v233, v231, v233\n"
    "v_accvgpr_write_b32 a55, v233\n"
    "v_accvgpr_read_b32 v232, a64\n"
    "v_mul_f32_e32 v232, v228, v232\n"
    "v_accvgpr_write_b32 a64, v232\n"
    "v_accvgpr_read_b32 v233, a65\n"
    "v_mul_f32_e32 v233, v229, v233\n"
    "v_accvgpr_write_b32 a65, v233\n"
    "v_accvgpr_read_b32 v232, a66\n"
    "v_mul_f32_e32 v232, v230, v232\n"
    "v_accvgpr_write_b32 a66, v232\n"
    "v_accvgpr_read_b32 v233, a67\n"
    "v_mul_f32_e32 v233, v231, v233\n"
    "v_accvgpr_write_b32 a67, v233\n"
    "v_accvgpr_read_b32 v232, a76\n"
    "v_mul_f32_e32 v232, v228, v232\n"
    "v_accvgpr_write_b32 a76, v232\n"
    "v_accvgpr_read_b32 v233, a77\n"
    "v_mul_f32_e32 v233, v229, v233\n"
    "v_accvgpr_write_b32 a77, v233\n"
    "v_accvgpr_read_b32 v232, a78\n"
    "v_mul_f32_e32 v232, v230, v232\n"
    "v_accvgpr_write_b32 a78, v232\n"
    "v_accvgpr_read_b32 v233, a79\n"
    "v_mul_f32_e32 v233, v231, v233\n"
    "v_accvgpr_write_b32 a79, v233\n"
    "v_accvgpr_read_b32 v232, a88\n"
    "v_mul_f32_e32 v232, v228, v232\n"
    "v_accvgpr_write_b32 a88, v232\n"
    "v_accvgpr_read_b32 v233, a89\n"
    "v_mul_f32_e32 v233, v229, v233\n"
    "v_accvgpr_write_b32 a89, v233\n"
    "v_accvgpr_read_b32 v232, a90\n"
    "v_mul_f32_e32 v232, v230, v232\n"
    "v_accvgpr_write_b32 a90, v232\n"
    "v_accvgpr_read_b32 v233, a91\n"
    "v_mul_f32_e32 v233, v231, v233\n"
    "v_accvgpr_write_b32 a91, v233\n"
    "v_accvgpr_read_b32 v232, a100\n"
    "v_mul_f32_e32 v232, v228, v232\n"
    "v_accvgpr_write_b32 a100, v232\n"
    "v_accvgpr_read_b32 v233, a101\n"
    "v_mul_f32_e32 v233, v229, v233\n"
    "v_accvgpr_write_b32 a101, v233\n"
    "v_accvgpr_read_b32 v232, a102\n"
    "v_mul_f32_e32 v232, v230, v232\n"
    "v_accvgpr_write_b32 a102, v232\n"
    "v_accvgpr_read_b32 v233, a103\n"
    "v_mul_f32_e32 v233, v231, v233\n"
    "v_accvgpr_write_b32 a103, v233\n"
    "v_accvgpr_read_b32 v232, a112\n"
    "v_mul_f32_e32 v232, v228, v232\n"
    "v_accvgpr_write_b32 a112, v232\n"
    "v_accvgpr_read_b32 v233, a113\n"
    "v_mul_f32_e32 v233, v229, v233\n"
    "v_accvgpr_write_b32 a113, v233\n"
    "v_accvgpr_read_b32 v232, a114\n"
    "v_mul_f32_e32 v232, v230, v232\n"
    "v_accvgpr_write_b32 a114, v232\n"
    "v_accvgpr_read_b32 v233, a115\n"
    "v_mul_f32_e32 v233, v231, v233\n"
    "v_accvgpr_write_b32 a115, v233\n"
    "v_accvgpr_read_b32 v232, a124\n"
    "v_mul_f32_e32 v232, v228, v232\n"
    "v_accvgpr_write_b32 a124, v232\n"
    "v_accvgpr_read_b32 v233, a125\n"
    "v_mul_f32_e32 v233, v229, v233\n"
    "v_accvgpr_write_b32 a125, v233\n"
    "v_accvgpr_read_b32 v232, a126\n"
    "v_mul_f32_e32 v232, v230, v232\n"
    "v_accvgpr_write_b32 a126, v232\n"
    "v_accvgpr_read_b32 v233, a127\n"
    "v_mul_f32_e32 v233, v231, v233\n"
    "v_accvgpr_write_b32 a127, v233\n"
    "v_accvgpr_read_b32 v232, a136\n"
    "v_mul_f32_e32 v232, v228, v232\n"
    "v_accvgpr_write_b32 a136, v232\n"
    "v_accvgpr_read_b32 v233, a137\n"
    "v_mul_f32_e32 v233, v229, v233\n"
    "v_accvgpr_write_b32 a137, v233\n"
    "v_accvgpr_read_b32 v232, a138\n"
    "v_mul_f32_e32 v232, v230, v232\n"
    "v_accvgpr_write_b32 a138, v232\n"
    "v_accvgpr_read_b32 v233, a139\n"
    "v_mul_f32_e32 v233, v231, v233\n"
    "v_accvgpr_write_b32 a139, v233\n"
    "v_accvgpr_read_b32 v232, a148\n"
    "v_mul_f32_e32 v232, v228, v232\n"
    "v_accvgpr_write_b32 a148, v232\n"
    "v_accvgpr_read_b32 v233, a149\n"
    "v_mul_f32_e32 v233, v229, v233\n"
    "v_accvgpr_write_b32 a149, v233\n"
    "v_accvgpr_read_b32 v232, a150\n"
    "v_mul_f32_e32 v232, v230, v232\n"
    "v_accvgpr_write_b32 a150, v232\n"
    "v_accvgpr_read_b32 v233, a151\n"
    "v_mul_f32_e32 v233, v231, v233\n"
    "v_accvgpr_write_b32 a151, v233\n"
    "v_accvgpr_read_b32 v232, a160\n"
    "v_mul_f32_e32 v232, v228, v232\n"
    "v_accvgpr_write_b32 a160, v232\n"
    "v_accvgpr_read_b32 v233, a161\n"
    "v_mul_f32_e32 v233, v229, v233\n"
    "v_accvgpr_write_b32 a161, v233\n"
    "v_accvgpr_read_b32 v232, a162\n"
    "v_mul_f32_e32 v232, v230, v232\n"
    "v_accvgpr_write_b32 a162, v232\n"
    "v_accvgpr_read_b32 v233, a163\n"
    "v_mul_f32_e32 v233, v231, v233\n"
    "v_accvgpr_write_b32 a163, v233\n"
    "v_accvgpr_read_b32 v232, a172\n"
    "v_mul_f32_e32 v232, v228, v232\n"
    "v_accvgpr_write_b32 a172, v232\n"
    "v_accvgpr_read_b32 v233, a173\n"
    "v_mul_f32_e32 v233, v229, v233\n"
    "v_accvgpr_write_b32 a173, v233\n"
    "v_accvgpr_read_b32 v232, a174\n"
    "v_mul_f32_e32 v232, v230, v232\n"
    "v_accvgpr_write_b32 a174, v232\n"
    "v_accvgpr_read_b32 v233, a175\n"
    "v_mul_f32_e32 v233, v231, v233\n"
    "v_accvgpr_write_b32 a175, v233\n"
    "v_accvgpr_read_b32 v232, a184\n"
    "v_mul_f32_e32 v232, v228, v232\n"
    "v_accvgpr_write_b32 a184, v232\n"
    "v_accvgpr_read_b32 v233, a185\n"
    "v_mul_f32_e32 v233, v229, v233\n"
    "v_accvgpr_write_b32 a185, v233\n"
    "v_accvgpr_read_b32 v232, a186\n"
    "v_mul_f32_e32 v232, v230, v232\n"
    "v_accvgpr_write_b32 a186, v232\n"
    "v_accvgpr_read_b32 v233, a187\n"
    "v_mul_f32_e32 v233, v231, v233\n"
    "v_accvgpr_write_b32 a187, v233\n"
    "v_max3_f32 v251, v8, v9, v10\n"
    "v_max3_f32 v251, v251, v11, v20\n"
    "v_max3_f32 v251, v251, v21, v22\n"
    "v_max3_f32 v251, v251, v23, v32\n"
    "v_max3_f32 v251, v251, v33, v34\n"
    "v_max3_f32 v251, v251, v35, v44\n"
    "v_max3_f32 v251, v251, v45, v46\n"
    "v_max_f32_e32 v251, v251, v47\n"
    "v_mov_b32_e32 v252, v251\n"
    "s_nop 1\n"
    "v_permlane16_swap_b32_e32 v252, v251\n"
    "s_nop 0\n"
    "v_max_f32_e32 v251, v251, v252\n"
    "v_mov_b32_e32 v252, v251\n"
    "s_nop 1\n"
    "v_permlane32_swap_b32_e32 v252, v251\n"
    "s_nop 0\n"
    "v_max_f32_e32 v251, v251, v252\n"
    "v_max_f32_e32 v251, 0, v251\n"
    "v_sub_f32_e32 v252, 0, v251\n"
    "v_exp_f32_e32 v252, v252\n"
    "s_nop 0\n"
    "v_mul_f32_e32 v239, v252, v239\n"
    "v_mbcnt_lo_u32_b32 v253, -1, 0\n"
    "v_mbcnt_hi_u32_b32 v253, -1, v253\n"
    "v_and_b32_e32 v253, 48, v253\n"
    "ds_bpermute_b32 v228, v253, v252 offset:0\n"
    "ds_bpermute_b32 v229, v253, v252 offset:4\n"
    "ds_bpermute_b32 v230, v253, v252 offset:8\n"
    "ds_bpermute_b32 v231, v253, v252 offset:12\n"
    "v_sub_f32_e32 v224, v224, v251\n"
    "v_sub_f32_e32 v225, v225, v251\n"
    "v_sub_f32_e32 v226, v226, v251\n"
    "v_sub_f32_e32 v227, v227, v251\n"
    "v_sub_f32_e32 v8, v8, v251\n"
    "v_exp_f32_e32 v8, v8\n"
    "v_sub_f32_e32 v56, v56, v251\n"
    "v_sub_f32_e32 v9, v9, v251\n"
    "v_exp_f32_e32 v9, v9\n"
    "v_sub_f32_e32 v57, v57, v251\n"
    "v_sub_f32_e32 v10, v10, v251\n"
    "v_exp_f32_e32 v10, v10\n"
    "v_sub_f32_e32 v58, v58, v251\n"
    "v_sub_f32_e32 v11, v11, v251\n"
    "v_exp_f32_e32 v11, v11\n"
    "v_sub_f32_e32 v59, v59, v251\n"
    "v_sub_f32_e32 v20, v20, v251\n"
    "v_exp_f32_e32 v20, v20\n"
    "v_sub_f32_e32 v68, v68, v251\n"
    "v_sub_f32_e32 v21, v21, v251\n"
    "v_exp_f32_e32 v21, v21\n"
    "v_sub_f32_e32 v69, v69, v251\n"
    "v_sub_f32_e32 v22, v22, v251\n"
    "v_exp_f32_e32 v22, v22\n"
    "v_sub_f32_e32 v70, v70, v251\n"
    "v_sub_f32_e32 v23, v23, v251\n"
    "v_exp_f32_e32 v23, v23\n"
    "v_sub_f32_e32 v71, v71, v251\n"
    "v_sub_f32_e32 v32, v32, v251\n"
    "v_exp_f32_e32 v32, v32\n"
    "v_sub_f32_e32 v80, v80, v251\n"
    "v_sub_f32_e32 v33, v33, v251\n"
    "v_exp_f32_e32 v33, v33\n"
    "v_sub_f32_e32 v81, v81, v251\n"
    "v_sub_f32_e32 v34, v34, v251\n"
    "v_exp_f32_e32 v34, v34\n"
    "v_sub_f32_e32 v82, v82, v251\n"
    "v_sub_f32_e32 v35, v35, v251\n"
    "v_exp_f32_e32 v35, v35\n"
    "v_sub_f32_e32 v83, v83, v251\n"
    "v_sub_f32_e32 v44, v44, v251\n"
    "v_exp_f32_e32 v44, v44\n"
    "v_sub_f32_e32 v92, v92, v251\n"
    "v_sub_f32_e32 v45, v45, v251\n"
    "v_exp_f32_e32 v45, v45\n"
    "v_sub_f32_e32 v93, v93, v251\n"
    "v_sub_f32_e32 v46, v46, v251\n"
    "v_exp_f32_e32 v46, v46\n"
    "v_sub_f32_e32 v94, v94, v251\n"
    "v_sub_f32_e32 v47, v47, v251\n"
    "v_exp_f32_e32 v47, v47\n"
    "v_sub_f32_e32 v95, v95, v251\n"
    "s_nop 0\n"
    "v_add_f32_e32 v236, v8, v9\n"
    "v_add_f32_e32 v236, v10, v236\n"
    "v_add_f32_e32 v236, v11, v236\n"
    "v_add_f32_e32 v236, v20, v236\n"
    "v_add_f32_e32 v236, v21, v236\n"
    "v_add_f32_e32 v236, v22, v236\n"
    "v_add_f32_e32 v236, v23, v236\n"
    "v_add_f32_e32 v236, v32, v236\n"
    "v_add_f32_e32 v236, v33, v236\n"
    "v_add_f32_e32 v236, v34, v236\n"
    "v_add_f32_e32 v236, v35, v236\n"
    "v_add_f32_e32 v236, v44, v236\n"
    "v_add_f32_e32 v236, v45, v236\n"
    "v_add_f32_e32 v236, v46, v236\n"
    "v_add_f32_e32 v236, v47, v236\n"
    "v_cvt_pk_bf16_f32 v112, v8, v9\n"
    "v_cvt_pk_bf16_f32 v113, v10, v11\n"
    "v_cvt_pk_bf16_f32 v114, v20, v21\n"
    "v_cvt_pk_bf16_f32 v115, v22, v23\n"
    "v_cvt_pk_bf16_f32 v116, v32, v33\n"
    "v_cvt_pk_bf16_f32 v117, v34, v35\n"
    "v_cvt_pk_bf16_f32 v118, v44, v45\n"
    "v_cvt_pk_bf16_f32 v119, v46, v47\n"
    "s_waitcnt lgkmcnt(0)\n"
    "v_accvgpr_read_b32 v232, a8\n"
    "v_mul_f32_e32 v232, v228, v232\n"
    "v_accvgpr_write_b32 a8, v232\n"
    "v_accvgpr_read_b32 v233, a9\n"
    "v_mul_f32_e32 v233, v229, v233\n"
    "v_accvgpr_write_b32 a9, v233\n"
    "v_accvgpr_read_b32 v232, a10\n"
    "v_mul_f32_e32 v232, v230, v232\n"
    "v_accvgpr_write_b32 a10, v232\n"
    "v_accvgpr_read_b32 v233, a11\n"
    "v_mul_f32_e32 v233, v231, v233\n"
    "v_accvgpr_write_b32 a11, v233\n"
    "v_accvgpr_read_b32 v232, a20\n"
    "v_mul_f32_e32 v232, v228, v232\n"
    "v_accvgpr_write_b32 a20, v232\n"
    "v_accvgpr_read_b32 v233, a21\n"
    "v_mul_f32_e32 v233, v229, v233\n"
    "v_accvgpr_write_b32 a21, v233\n"
    "v_accvgpr_read_b32 v232, a22\n"
    "v_mul_f32_e32 v232, v230, v232\n"
    "v_accvgpr_write_b32 a22, v232\n"
    "v_accvgpr_read_b32 v233, a23\n"
    "v_mul_f32_e32 v233, v231, v233\n"
    "v_accvgpr_write_b32 a23, v233\n"
    "v_accvgpr_read_b32 v232, a32\n"
    "v_mul_f32_e32 v232, v228, v232\n"
    "v_accvgpr_write_b32 a32, v232\n"
    "v_accvgpr_read_b32 v233, a33\n"
    "v_mul_f32_e32 v233, v229, v233\n"
    "v_accvgpr_write_b32 a33, v233\n"
    "v_accvgpr_read_b32 v232, a34\n"
    "v_mul_f32_e32 v232, v230, v232\n"
    "v_accvgpr_write_b32 a34, v232\n"
    "v_accvgpr_read_b32 v233, a35\n"
    "v_mul_f32_e32 v233, v231, v233\n"
    "v_accvgpr_write_b32 a35, v233\n"
    "v_accvgpr_read_b32 v232, a44\n"
    "v_mul_f32_e32 v232, v228, v232\n"
    "v_accvgpr_write_b32 a44, v232\n"
    "v_accvgpr_read_b32 v233, a45\n"
    "v_mul_f32_e32 v233, v229, v233\n"
    "v_accvgpr_write_b32 a45, v233\n"
    "v_accvgpr_read_b32 v232, a46\n"
    "v_mul_f32_e32 v232, v230, v232\n"
    "v_accvgpr_write_b32 a46, v232\n"
    "v_accvgpr_read_b32 v233, a47\n"
    "v_mul_f32_e32 v233, v231, v233\n"
    "v_accvgpr_write_b32 a47, v233\n"
    "v_accvgpr_read_b32 v232, a56\n"
    "v_mul_f32_e32 v232, v228, v232\n"
    "v_accvgpr_write_b32 a56, v232\n"
    "v_accvgpr_read_b32 v233, a57\n"
    "v_mul_f32_e32 v233, v229, v233\n"
    "v_accvgpr_write_b32 a57, v233\n"
    "v_accvgpr_read_b32 v232, a58\n"
    "v_mul_f32_e32 v232, v230, v232\n"
    "v_accvgpr_write_b32 a58, v232\n"
    "v_accvgpr_read_b32 v233, a59\n"
    "v_mul_f32_e32 v233, v231, v233\n"
    "v_accvgpr_write_b32 a59, v233\n"
    "v_accvgpr_read_b32 v232, a68\n"
    "v_mul_f32_e32 v232, v228, v232\n"
    "v_accvgpr_write_b32 a68, v232\n"
    "v_accvgpr_read_b32 v233, a69\n"
    "v_mul_f32_e32 v233, v229, v233\n"
    "v_accvgpr_write_b32 a69, v233\n"
    "v_accvgpr_read_b32 v232, a70\n"
    "v_mul_f32_e32 v232, v230, v232\n"
    "v_accvgpr_write_b32 a70, v232\n"
    "v_accvgpr_read_b32 v233, a71\n"
    "v_mul_f32_e32 v233, v231, v233\n"
    "v_accvgpr_write_b32 a71, v233\n"
    "v_accvgpr_read_b32 v232, a80\n"
    "v_mul_f32_e32 v232, v228, v232\n"
    "v_accvgpr_write_b32 a80, v232\n"
    "v_accvgpr_read_b32 v233, a81\n"
    "v_mul_f32_e32 v233, v229, v233\n"
    "v_accvgpr_write_b32 a81, v233\n"
    "v_accvgpr_read_b32 v232, a82\n"
    "v_mul_f32_e32 v232, v230, v232\n"
    "v_accvgpr_write_b32 a82, v232\n"
    "v_accvgpr_read_b32 v233, a83\n"
    "v_mul_f32_e32 v233, v231, v233\n"
    "v_accvgpr_write_b32 a83, v233\n"
    "v_accvgpr_read_b32 v232, a92\n"
    "v_mul_f32_e32 v232, v228, v232\n"
    "v_accvgpr_write_b32 a92, v232\n"
    "v_accvgpr_read_b32 v233, a93\n"
    "v_mul_f32_e32 v233, v229, v233\n"
    "v_accvgpr_write_b32 a93, v233\n"
    "v_accvgpr_read_b32 v232, a94\n"
    "v_mul_f32_e32 v232, v230, v232\n"
    "v_accvgpr_write_b32 a94, v232\n"
    "v_accvgpr_read_b32 v233, a95\n"
    "v_mul_f32_e32 v233, v231, v233\n"
    "v_accvgpr_write_b32 a95, v233\n"
    "v_accvgpr_read_b32 v232, a104\n"
    "v_mul_f32_e32 v232, v228, v232\n"
    "v_accvgpr_write_b32 a104, v232\n"
    "v_accvgpr_read_b32 v233, a105\n"
    "v_mul_f32_e32 v233, v229, v233\n"
    "v_accvgpr_write_b32 a105, v233\n"
    "v_accvgpr_read_b32 v232, a106\n"
    "v_mul_f32_e32 v232, v230, v232\n"
    "v_accvgpr_write_b32 a106, v232\n"
    "v_accvgpr_read_b32 v233, a107\n"
    "v_mul_f32_e32 v233, v231, v233\n"
    "v_accvgpr_write_b32 a107, v233\n"
    "v_accvgpr_read_b32 v232, a116\n"
    "v_mul_f32_e32 v232, v228, v232\n"
    "v_accvgpr_write_b32 a116, v232\n"
    "v_accvgpr_read_b32 v233, a117\n"
    "v_mul_f32_e32 v233, v229, v233\n"
    "v_accvgpr_write_b32 a117, v233\n"
    "v_accvgpr_read_b32 v232, a118\n"
    "v_mul_f32_e32 v232, v230, v232\n"
    "v_accvgpr_write_b32 a118, v232\n"
    "v_accvgpr_read_b32 v233, a119\n"
    "v_mul_f32_e32 v233, v231, v233\n"
    "v_accvgpr_write_b32 a119, v233\n"
    "v_accvgpr_read_b32 v232, a128\n"
    "v_mul_f32_e32 v232, v228, v232\n"
    "v_accvgpr_write_b32 a128, v232\n"
    "v_accvgpr_read_b32 v233, a129\n"
    "v_mul_f32_e32 v233, v229, v233\n"
    "v_accvgpr_write_b32 a129, v233\n"
    "v_accvgpr_read_b32 v232, a130\n"
    "v_mul_f32_e32 v232, v230, v232\n"
    "v_accvgpr_write_b32 a130, v232\n"
    "v_accvgpr_read_b32 v233, a131\n"
    "v_mul_f32_e32 v233, v231, v233\n"
    "v_accvgpr_write_b32 a131, v233\n"
    "v_accvgpr_read_b32 v232, a140\n"
    "v_mul_f32_e32 v232, v228, v232\n"
    "v_accvgpr_write_b32 a140, v232\n"
    "v_accvgpr_read_b32 v233, a141\n"
    "v_mul_f32_e32 v233, v229, v233\n"
    "v_accvgpr_write_b32 a141, v233\n"
    "v_accvgpr_read_b32 v232, a142\n"
    "v_mul_f32_e32 v232, v230, v232\n"
    "v_accvgpr_write_b32 a142, v232\n"
    "v_accvgpr_read_b32 v233, a143\n"
    "v_mul_f32_e32 v233, v231, v233\n"
    "v_accvgpr_write_b32 a143, v233\n"
    "v_accvgpr_read_b32 v232, a152\n"
    "v_mul_f32_e32 v232, v228, v232\n"
    "v_accvgpr_write_b32 a152, v232\n"
    "v_accvgpr_read_b32 v233, a153\n"
    "v_mul_f32_e32 v233, v229, v233\n"
    "v_accvgpr_write_b32 a153, v233\n"
    "v_accvgpr_read_b32 v232, a154\n"
    "v_mul_f32_e32 v232, v230, v232\n"
    "v_accvgpr_write_b32 a154, v232\n"
    "v_accvgpr_read_b32 v233, a155\n"
    "v_mul_f32_e32 v233, v231, v233\n"
    "v_accvgpr_write_b32 a155, v233\n"
    "v_accvgpr_read_b32 v232, a164\n"
    "v_mul_f32_e32 v232, v228, v232\n"
    "v_accvgpr_write_b32 a164, v232\n"
    "v_accvgpr_read_b32 v233, a165\n"
    "v_mul_f32_e32 v233, v229, v233\n"
    "v_accvgpr_write_b32 a165, v233\n"
    "v_accvgpr_read_b32 v232, a166\n"
    "v_mul_f32_e32 v232, v230, v232\n"
    "v_accvgpr_write_b32 a166, v232\n"
    "v_accvgpr_read_b32 v233, a167\n"
    "v_mul_f32_e32 v233, v231, v233\n"
    "v_accvgpr_write_b32 a167, v233\n"
    "v_accvgpr_read_b32 v232, a176\n"
    "v_mul_f32_e32 v232, v228, v232\n"
    "v_accvgpr_write_b32 a176, v232\n"
    "v_accvgpr_read_b32 v233, a177\n"
    "v_mul_f32_e32 v233, v229, v233\n"
    "v_accvgpr_write_b32 a177, v233\n"
    "v_accvgpr_read_b32 v232, a178\n"
    "v_mul_f32_e32 v232, v230, v232\n"
    "v_accvgpr_write_b32 a178, v232\n"
    "v_accvgpr_read_b32 v233, a179\n"
    "v_mul_f32_e32 v233, v231, v233\n"
    "v_accvgpr_write_b32 a179, v233\n"
    "v_accvgpr_read_b32 v232, a188\n"
    "v_mul_f32_e32 v232, v228, v232\n"
    "v_accvgpr_write_b32 a188, v232\n"
    "v_accvgpr_read_b32 v233, a189\n"
    "v_mul_f32_e32 v233, v229, v233\n"
    "v_accvgpr_write_b32 a189, v233\n"
    "v_accvgpr_read_b32 v232, a190\n"
    "v_mul_f32_e32 v232, v230, v232\n"
    "v_accvgpr_write_b32 a190, v232\n"
    "v_accvgpr_read_b32 v233, a191\n"
    "v_mul_f32_e32 v233, v231, v233\n"
    "v_accvgpr_write_b32 a191, v233\n"
    "s_nop 1\n"
    "s_branch RET1_%=\n"
    "RARET_%=:\n"
    "s_nop 7\n"
    "s_waitcnt lgkmcnt(0)\n"
    "v_max3_f32 v251, v48, v49, v50\n"
    "v_max3_f32 v251, v251, v51, v60\n"
    "v_max3_f32 v251, v251, v61, v62\n"
    "v_max3_f32 v251, v251, v63, v72\n"
    "v_max3_f32 v251, v251, v73, v74\n"
    "v_max3_f32 v251, v251, v75, v84\n"
    "v_max3_f32 v251, v251, v85, v86\n"
    "v_max_f32_e32 v251, v251, v87\n"
    "v_mov_b32_e32 v252, v251\n"
    "s_nop 1\n"
    "v_permlane16_swap_b32_e32 v252, v251\n"
    "s_nop 0\n"
    "v_max_f32_e32 v251, v251, v252\n"
    "v_mov_b32_e32 v252, v251\n"
    "s_nop 1\n"
    "v_permlane32_swap_b32_e32 v252, v251\n"
    "s_nop 0\n"
    "v_max_f32_e32 v251, v251, v252\n"
    "v_max_f32_e32 v251, 0, v251\n"
    "v_sub_f32_e32 v252, 0, v251\n"
    "v_exp_f32_e32 v252, v252\n"
    "s_nop 0\n"
    "v_mul_f32_e32 v237, v252, v237\n"
    "v_mbcnt_lo_u32_b32 v253, -1, 0\n"
    "v_mbcnt_hi_u32_b32 v253, -1, v253\n"
    "v_and_b32_e32 v253, 48, v253\n"
    "ds_bpermute_b32 v228, v253, v252 offset:0\n"
    "ds_bpermute_b32 v229, v253, v252 offset:4\n"
    "ds_bpermute_b32 v230, v253, v252 offset:8\n"
    "ds_bpermute_b32 v231, v253, v252 offset:12\n"
    "v_sub_f32_e32 v216, v216, v251\n"
    "v_sub_f32_e32 v217, v217, v251\n"
    "v_sub_f32_e32 v218, v218, v251\n"
    "v_sub_f32_e32 v219, v219, v251\n"
    "v_sub_f32_e32 v48, v48, v251\n"
    "v_exp_f32_e32 v48, v48\n"
    "v_sub_f32_e32 v49, v49, v251\n"
    "v_exp_f32_e32 v49, v49\n"
    "v_sub_f32_e32 v50, v50, v251\n"
    "v_exp_f32_e32 v50, v50\n"
    "v_sub_f32_e32 v51, v51, v251\n"
    "v_exp_f32_e32 v51, v51\n"
    "v_sub_f32_e32 v60, v60, v251\n"
    "v_exp_f32_e32 v60, v60\n"
    "v_sub_f32_e32 v61, v61, v251\n"
    "v_exp_f32_e32 v61, v61\n"
    "v_sub_f32_e32 v62, v62, v251\n"
    "v_exp_f32_e32 v62, v62\n"
    "v_sub_f32_e32 v63, v63, v251\n"
    "v_exp_f32_e32 v63, v63\n"
    "v_sub_f32_e32 v72, v72, v251\n"
    "v_exp_f32_e32 v72, v72\n"
    "v_sub_f32_e32 v73, v73, v251\n"
    "v_exp_f32_e32 v73, v73\n"
    "v_sub_f32_e32 v74, v74, v251\n"
    "v_exp_f32_e32 v74, v74\n"
    "v_sub_f32_e32 v75, v75, v251\n"
    "v_exp_f32_e32 v75, v75\n"
    "v_sub_f32_e32 v84, v84, v251\n"
    "v_exp_f32_e32 v84, v84\n"
    "v_sub_f32_e32 v85, v85, v251\n"
    "v_exp_f32_e32 v85, v85\n"
    "v_sub_f32_e32 v86, v86, v251\n"
    "v_exp_f32_e32 v86, v86\n"
    "v_sub_f32_e32 v87, v87, v251\n"
    "v_exp_f32_e32 v87, v87\n"
    "s_nop 0\n"
    "v_add_f32_e32 v234, v48, v49\n"
    "v_add_f32_e32 v234, v50, v234\n"
    "v_add_f32_e32 v234, v51, v234\n"
    "v_add_f32_e32 v234, v60, v234\n"
    "v_add_f32_e32 v234, v61, v234\n"
    "v_add_f32_e32 v234, v62, v234\n"
    "v_add_f32_e32 v234, v63, v234\n"
    "v_add_f32_e32 v234, v72, v234\n"
    "v_add_f32_e32 v234, v73, v234\n"
    "v_add_f32_e32 v234, v74, v234\n"
    "v_add_f32_e32 v234, v75, v234\n"
    "v_add_f32_e32 v234, v84, v234\n"
    "v_add_f32_e32 v234, v85, v234\n"
    "v_add_f32_e32 v234, v86, v234\n"
    "v_add_f32_e32 v234, v87, v234\n"
    "v_cvt_pk_bf16_f32 v120, v48, v49\n"
    "v_cvt_pk_bf16_f32 v121, v50, v51\n"
    "v_cvt_pk_bf16_f32 v122, v60, v61\n"
    "v_cvt_pk_bf16_f32 v123, v62, v63\n"
    "v_cvt_pk_bf16_f32 v124, v72, v73\n"
    "v_cvt_pk_bf16_f32 v125, v74, v75\n"
    "v_cvt_pk_bf16_f32 v126, v84, v85\n"
    "v_cvt_pk_bf16_f32 v127, v86, v87\n"
    "s_waitcnt lgkmcnt(0)\n"
    "v_accvgpr_read_b32 v232, a0\n"
    "v_mul_f32_e32 v232, v228, v232\n"
    "v_accvgpr_write_b32 a0, v232\n"
    "v_accvgpr_read_b32 v233, a1\n"
    "v_mul_f32_e32 v233, v229, v233\n"
    "v_accvgpr_write_b32 a1, v233\n"
    "v_accvgpr_read_b32 v232, a2\n"
    "v_mul_f32_e32 v232, v230, v232\n"
    "v_accvgpr_write_b32 a2, v232\n"
    "v_accvgpr_read_b32 v233, a3\n"
    "v_mul_f32_e32 v233, v231, v233\n"
    "v_accvgpr_write_b32 a3, v233\n"
    "v_accvgpr_read_b32 v232, a12\n"
    "v_mul_f32_e32 v232, v228, v232\n"
    "v_accvgpr_write_b32 a12, v232\n"
    "v_accvgpr_read_b32 v233, a13\n"
    "v_mul_f32_e32 v233, v229, v233\n"
    "v_accvgpr_write_b32 a13, v233\n"
    "v_accvgpr_read_b32 v232, a14\n"
    "v_mul_f32_e32 v232, v230, v232\n"
    "v_accvgpr_write_b32 a14, v232\n"
    "v_accvgpr_read_b32 v233, a15\n"
    "v_mul_f32_e32 v233, v231, v233\n"
    "v_accvgpr_write_b32 a15, v233\n"
    "v_accvgpr_read_b32 v232, a24\n"
    "v_mul_f32_e32 v232, v228, v232\n"
    "v_accvgpr_write_b32 a24, v232\n"
    "v_accvgpr_read_b32 v233, a25\n"
    "v_mul_f32_e32 v233, v229, v233\n"
    "v_accvgpr_write_b32 a25, v233\n"
    "v_accvgpr_read_b32 v232, a26\n"
    "v_mul_f32_e32 v232, v230, v232\n"
    "v_accvgpr_write_b32 a26, v232\n"
    "v_accvgpr_read_b32 v233, a27\n"
    "v_mul_f32_e32 v233, v231, v233\n"
    "v_accvgpr_write_b32 a27, v233\n"
    "v_accvgpr_read_b32 v232, a36\n"
    "v_mul_f32_e32 v232, v228, v232\n"
    "v_accvgpr_write_b32 a36, v232\n"
    "v_accvgpr_read_b32 v233, a37\n"
    "v_mul_f32_e32 v233, v229, v233\n"
    "v_accvgpr_write_b32 a37, v233\n"
    "v_accvgpr_read_b32 v232, a38\n"
    "v_mul_f32_e32 v232, v230, v232\n"
    "v_accvgpr_write_b32 a38, v232\n"
    "v_accvgpr_read_b32 v233, a39\n"
    "v_mul_f32_e32 v233, v231, v233\n"
    "v_accvgpr_write_b32 a39, v233\n"
    "v_accvgpr_read_b32 v232, a48\n"
    "v_mul_f32_e32 v232, v228, v232\n"
    "v_accvgpr_write_b32 a48, v232\n"
    "v_accvgpr_read_b32 v233, a49\n"
    "v_mul_f32_e32 v233, v229, v233\n"
    "v_accvgpr_write_b32 a49, v233\n"
    "v_accvgpr_read_b32 v232, a50\n"
    "v_mul_f32_e32 v232, v230, v232\n"
    "v_accvgpr_write_b32 a50, v232\n"
    "v_accvgpr_read_b32 v233, a51\n"
    "v_mul_f32_e32 v233, v231, v233\n"
    "v_accvgpr_write_b32 a51, v233\n"
    "v_accvgpr_read_b32 v232, a60\n"
    "v_mul_f32_e32 v232, v228, v232\n"
    "v_accvgpr_write_b32 a60, v232\n"
    "v_accvgpr_read_b32 v233, a61\n"
    "v_mul_f32_e32 v233, v229, v233\n"
    "v_accvgpr_write_b32 a61, v233\n"
    "v_accvgpr_read_b32 v232, a62\n"
    "v_mul_f32_e32 v232, v230, v232\n"
    "v_accvgpr_write_b32 a62, v232\n"
    "v_accvgpr_read_b32 v233, a63\n"
    "v_mul_f32_e32 v233, v231, v233\n"
    "v_accvgpr_write_b32 a63, v233\n"
    "v_accvgpr_read_b32 v232, a72\n"
    "v_mul_f32_e32 v232, v228, v232\n"
    "v_accvgpr_write_b32 a72, v232\n"
    "v_accvgpr_read_b32 v233, a73\n"
    "v_mul_f32_e32 v233, v229, v233\n"
    "v_accvgpr_write_b32 a73, v233\n"
    "v_accvgpr_read_b32 v232, a74\n"
    "v_mul_f32_e32 v232, v230, v232\n"
    "v_accvgpr_write_b32 a74, v232\n"
    "v_accvgpr_read_b32 v233, a75\n"
    "v_mul_f32_e32 v233, v231, v233\n"
    "v_accvgpr_write_b32 a75, v233\n"
    "v_accvgpr_read_b32 v232, a84\n"
    "v_mul_f32_e32 v232, v228, v232\n"
    "v_accvgpr_write_b32 a84, v232\n"
    "v_accvgpr_read_b32 v233, a85\n"
    "v_mul_f32_e32 v233, v229, v233\n"
    "v_accvgpr_write_b32 a85, v233\n"
    "v_accvgpr_read_b32 v232, a86\n"
    "v_mul_f32_e32 v232, v230, v232\n"
    "v_accvgpr_write_b32 a86, v232\n"
    "v_accvgpr_read_b32 v233, a87\n"
    "v_mul_f32_e32 v233, v231, v233\n"
    "v_accvgpr_write_b32 a87, v233\n"
    "v_accvgpr_read_b32 v232, a96\n"
    "v_mul_f32_e32 v232, v228, v232\n"
    "v_accvgpr_write_b32 a96, v232\n"
    "v_accvgpr_read_b32 v233, a97\n"
    "v_mul_f32_e32 v233, v229, v233\n"
    "v_accvgpr_write_b32 a97, v233\n"
    "v_accvgpr_read_b32 v232, a98\n"
    "v_mul_f32_e32 v232, v230, v232\n"
    "v_accvgpr_write_b32 a98, v232\n"
    "v_accvgpr_read_b32 v233, a99\n"
    "v_mul_f32_e32 v233, v231, v233\n"
    "v_accvgpr_write_b32 a99, v233\n"
    "v_accvgpr_read_b32 v232, a108\n"
    "v_mul_f32_e32 v232, v228, v232\n"
    "v_accvgpr_write_b32 a108, v232\n"
    "v_accvgpr_read_b32 v233, a109\n"
    "v_mul_f32_e32 v233, v229, v233\n"
    "v_accvgpr_write_b32 a109, v233\n"
    "v_accvgpr_read_b32 v232, a110\n"
    "v_mul_f32_e32 v232, v230, v232\n"
    "v_accvgpr_write_b32 a110, v232\n"
    "v_accvgpr_read_b32 v233, a111\n"
    "v_mul_f32_e32 v233, v231, v233\n"
    "v_accvgpr_write_b32 a111, v233\n"
    "v_accvgpr_read_b32 v232, a120\n"
    "v_mul_f32_e32 v232, v228, v232\n"
    "v_accvgpr_write_b32 a120, v232\n"
    "v_accvgpr_read_b32 v233, a121\n"
    "v_mul_f32_e32 v233, v229, v233\n"
    "v_accvgpr_write_b32 a121, v233\n"
    "v_accvgpr_read_b32 v232, a122\n"
    "v_mul_f32_e32 v232, v230, v232\n"
    "v_accvgpr_write_b32 a122, v232\n"
    "v_accvgpr_read_b32 v233, a123\n"
    "v_mul_f32_e32 v233, v231, v233\n"
    "v_accvgpr_write_b32 a123, v233\n"
    "v_accvgpr_read_b32 v232, a132\n"
    "v_mul_f32_e32 v232, v228, v232\n"
    "v_accvgpr_write_b32 a132, v232\n"
    "v_accvgpr_read_b32 v233, a133\n"
    "v_mul_f32_e32 v233, v229, v233\n"
    "v_accvgpr_write_b32 a133, v233\n"
    "v_accvgpr_read_b32 v232, a134\n"
    "v_mul_f32_e32 v232, v230, v232\n"
    "v_accvgpr_write_b32 a134, v232\n"
    "v_accvgpr_read_b32 v233, a135\n"
    "v_mul_f32_e32 v233, v231, v233\n"
    "v_accvgpr_write_b32 a135, v233\n"
    "v_accvgpr_read_b32 v232, a144\n"
    "v_mul_f32_e32 v232, v228, v232\n"
    "v_accvgpr_write_b32 a144, v232\n"
    "v_accvgpr_read_b32 v233, a145\n"
    "v_mul_f32_e32 v233, v229, v233\n"
    "v_accvgpr_write_b32 a145, v233\n"
    "v_accvgpr_read_b32 v232, a146\n"
    "v_mul_f32_e32 v232, v230, v232\n"
    "v_accvgpr_write_b32 a146, v232\n"
    "v_accvgpr_read_b32 v233, a147\n"
    "v_mul_f32_e32 v233, v231, v233\n"
    "v_accvgpr_write_b32 a147, v233\n"
    "v_accvgpr_read_b32 v232, a156\n"
    "v_mul_f32_e32 v232, v228, v232\n"
    "v_accvgpr_write_b32 a156, v232\n"
    "v_accvgpr_read_b32 v233, a157\n"
    "v_mul_f32_e32 v233, v229, v233\n"
    "v_accvgpr_write_b32 a157, v233\n"
    "v_accvgpr_read_b32 v232, a158\n"
    "v_mul_f32_e32 v232, v230, v232\n"
    "v_accvgpr_write_b32 a158, v232\n"
    "v_accvgpr_read_b32 v233, a159\n"
    "v_mul_f32_e32 v233, v231, v233\n"
    "v_accvgpr_write_b32 a159, v233\n"
    "v_accvgpr_read_b32 v232, a168\n"
    "v_mul_f32_e32 v232, v228, v232\n"
    "v_accvgpr_write_b32 a168, v232\n"
    "v_accvgpr_read_b32 v233, a169\n"
    "v_mul_f32_e32 v233, v229, v233\n"
    "v_accvgpr_write_b32 a169, v233\n"
    "v_accvgpr_read_b32 v232, a170\n"
    "v_mul_f32_e32 v232, v230, v232\n"
    "v_accvgpr_write_b32 a170, v232\n"
    "v_accvgpr_read_b32 v233, a171\n"
    "v_mul_f32_e32 v233, v231, v233\n"
    "v_accvgpr_write_b32 a171, v233\n"
    "v_accvgpr_read_b32 v232, a180\n"
    "v_mul_f32_e32 v232, v228, v232\n"
    "v_accvgpr_write_b32 a180, v232\n"
    "v_accvgpr_read_b32 v233, a181\n"
    "v_mul_f32_e32 v233, v229, v233\n"
    "v_accvgpr_write_b32 a181, v233\n"
    "v_accvgpr_read_b32 v232, a182\n"
    "v_mul_f32_e32 v232, v230, v232\n"
    "v_accvgpr_write_b32 a182, v232\n"
    "v_accvgpr_read_b32 v233, a183\n"
    "v_mul_f32_e32 v233, v231, v233\n"
    "v_accvgpr_write_b32 a183, v233\n"
    "v_max3_f32 v251, v52, v53, v54\n"
    "v_max3_f32 v251, v251, v55, v64\n"
    "v_max3_f32 v251, v251, v65, v66\n"
    "v_max3_f32 v251, v251, v67, v76\n"
    "v_max3_f32 v251, v251, v77, v78\n"
    "v_max3_f32 v251, v251, v79, v88\n"
    "v_max3_f32 v251, v251, v89, v90\n"
    "v_max_f32_e32 v251, v251, v91\n"
    "v_mov_b32_e32 v252, v251\n"
    "s_nop 1\n"
    "v_permlane16_swap_b32_e32 v252, v251\n"
    "s_nop 0\n"
    "v_max_f32_e32 v251, v251, v252\n"
    "v_mov_b32_e32 v252, v251\n"
    "s_nop 1\n"
    "v_permlane32_swap_b32_e32 v252, v251\n"
    "s_nop 0\n"
    "v_max_f32_e32 v251, v251, v252\n"
    "v_max_f32_e32 v251, 0, v251\n"
    "v_sub_f32_e32 v252, 0, v251\n"
    "v_exp_f32_e32 v252, v252\n"
    "s_nop 0\n"
    "v_mul_f32_e32 v238, v252, v238\n"
    "v_mbcnt_lo_u32_b32 v253, -1, 0\n"
    "v_mbcnt_hi_u32_b32 v253, -1, v253\n"
    "v_and_b32_e32 v253, 48, v253\n"
    "ds_bpermute_b32 v228, v253, v252 offset:0\n"
    "ds_bpermute_b32 v229, v253, v252 offset:4\n"
    "ds_bpermute_b32 v230, v253, v252 offset:8\n"
    "ds_bpermute_b32 v231, v253, v252 offset:12\n"
    "v_sub_f32_e32 v220, v220, v251\n"
    "v_sub_f32_e32 v221, v221, v251\n"
    "v_sub_f32_e32 v222, v222, v251\n"
    "v_sub_f32_e32 v223, v223, v251\n"
    "v_sub_f32_e32 v52, v52, v251\n"
    "v_exp_f32_e32 v52, v52\n"
    "v_sub_f32_e32 v53, v53, v251\n"
    "v_exp_f32_e32 v53, v53\n"
    "v_sub_f32_e32 v54, v54, v251\n"
    "v_exp_f32_e32 v54, v54\n"
    "v_sub_f32_e32 v55, v55, v251\n"
    "v_exp_f32_e32 v55, v55\n"
    "v_sub_f32_e32 v64, v64, v251\n"
    "v_exp_f32_e32 v64, v64\n"
    "v_sub_f32_e32 v65, v65, v251\n"
    "v_exp_f32_e32 v65, v65\n"
    "v_sub_f32_e32 v66, v66, v251\n"
    "v_exp_f32_e32 v66, v66\n"
    "v_sub_f32_e32 v67, v67, v251\n"
    "v_exp_f32_e32 v67, v67\n"
    "v_sub_f32_e32 v76, v76, v251\n"
    "v_exp_f32_e32 v76, v76\n"
    "v_sub_f32_e32 v77, v77, v251\n"
    "v_exp_f32_e32 v77, v77\n"
    "v_sub_f32_e32 v78, v78, v251\n"
    "v_exp_f32_e32 v78, v78\n"
    "v_sub_f32_e32 v79, v79, v251\n"
    "v_exp_f32_e32 v79, v79\n"
    "v_sub_f32_e32 v88, v88, v251\n"
    "v_exp_f32_e32 v88, v88\n"
    "v_sub_f32_e32 v89, v89, v251\n"
    "v_exp_f32_e32 v89, v89\n"
    "v_sub_f32_e32 v90, v90, v251\n"
    "v_exp_f32_e32 v90, v90\n"
    "v_sub_f32_e32 v91, v91, v251\n"
    "v_exp_f32_e32 v91, v91\n"
    "s_nop 0\n"
    "v_add_f32_e32 v235, v52, v53\n"
    "v_add_f32_e32 v235, v54, v235\n"
    "v_add_f32_e32 v235, v55, v235\n"
    "v_add_f32_e32 v235, v64, v235\n"
    "v_add_f32_e32 v235, v65, v235\n"
    "v_add_f32_e32 v235, v66, v235\n"
    "v_add_f32_e32 v235, v67, v235\n"
    "v_add_f32_e32 v235, v76, v235\n"
    "v_add_f32_e32 v235, v77, v235\n"
    "v_add_f32_e32 v235, v78, v235\n"
    "v_add_f32_e32 v235, v79, v235\n"
    "v_add_f32_e32 v235, v88, v235\n"
    "v_add_f32_e32 v235, v89, v235\n"
    "v_add_f32_e32 v235, v90, v235\n"
    "v_add_f32_e32 v235, v91, v235\n"
    "v_cvt_pk_bf16_f32 v128, v52, v53\n"
    "v_cvt_pk_bf16_f32 v129, v54, v55\n"
    "v_cvt_pk_bf16_f32 v130, v64, v65\n"
    "v_cvt_pk_bf16_f32 v131, v66, v67\n"
    "v_cvt_pk_bf16_f32 v132, v76, v77\n"
    "v_cvt_pk_bf16_f32 v133, v78, v79\n"
    "v_cvt_pk_bf16_f32 v134, v88, v89\n"
    "v_cvt_pk_bf16_f32 v135, v90, v91\n"
    "s_waitcnt lgkmcnt(0)\n"
    "v_accvgpr_read_b32 v232, a4\n"
    "v_mul_f32_e32 v232, v228, v232\n"
    "v_accvgpr_write_b32 a4, v232\n"
    "v_accvgpr_read_b32 v233, a5\n"
    "v_mul_f32_e32 v233, v229, v233\n"
    "v_accvgpr_write_b32 a5, v233\n"
    "v_accvgpr_read_b32 v232, a6\n"
    "v_mul_f32_e32 v232, v230, v232\n"
    "v_accvgpr_write_b32 a6, v232\n"
    "v_accvgpr_read_b32 v233, a7\n"
    "v_mul_f32_e32 v233, v231, v233\n"
    "v_accvgpr_write_b32 a7, v233\n"
    "v_accvgpr_read_b32 v232, a16\n"
    "v_mul_f32_e32 v232, v228, v232\n"
    "v_accvgpr_write_b32 a16, v232\n"
    "v_accvgpr_read_b32 v233, a17\n"
    "v_mul_f32_e32 v233, v229, v233\n"
    "v_accvgpr_write_b32 a17, v233\n"
    "v_accvgpr_read_b32 v232, a18\n"
    "v_mul_f32_e32 v232, v230, v232\n"
    "v_accvgpr_write_b32 a18, v232\n"
    "v_accvgpr_read_b32 v233, a19\n"
    "v_mul_f32_e32 v233, v231, v233\n"
    "v_accvgpr_write_b32 a19, v233\n"
    "v_accvgpr_read_b32 v232, a28\n"
    "v_mul_f32_e32 v232, v228, v232\n"
    "v_accvgpr_write_b32 a28, v232\n"
    "v_accvgpr_read_b32 v233, a29\n"
    "v_mul_f32_e32 v233, v229, v233\n"
    "v_accvgpr_write_b32 a29, v233\n"
    "v_accvgpr_read_b32 v232, a30\n"
    "v_mul_f32_e32 v232, v230, v232\n"
    "v_accvgpr_write_b32 a30, v232\n"
    "v_accvgpr_read_b32 v233, a31\n"
    "v_mul_f32_e32 v233, v231, v233\n"
    "v_accvgpr_write_b32 a31, v233\n"
    "v_accvgpr_read_b32 v232, a40\n"
    "v_mul_f32_e32 v232, v228, v232\n"
    "v_accvgpr_write_b32 a40, v232\n"
    "v_accvgpr_read_b32 v233, a41\n"
    "v_mul_f32_e32 v233, v229, v233\n"
    "v_accvgpr_write_b32 a41, v233\n"
    "v_accvgpr_read_b32 v232, a42\n"
    "v_mul_f32_e32 v232, v230, v232\n"
    "v_accvgpr_write_b32 a42, v232\n"
    "v_accvgpr_read_b32 v233, a43\n"
    "v_mul_f32_e32 v233, v231, v233\n"
    "v_accvgpr_write_b32 a43, v233\n"
    "v_accvgpr_read_b32 v232, a52\n"
    "v_mul_f32_e32 v232, v228, v232\n"
    "v_accvgpr_write_b32 a52, v232\n"
    "v_accvgpr_read_b32 v233, a53\n"
    "v_mul_f32_e32 v233, v229, v233\n"
    "v_accvgpr_write_b32 a53, v233\n"
    "v_accvgpr_read_b32 v232, a54\n"
    "v_mul_f32_e32 v232, v230, v232\n"
    "v_accvgpr_write_b32 a54, v232\n"
    "v_accvgpr_read_b32 v233, a55\n"
    "v_mul_f32_e32 v233, v231, v233\n"
    "v_accvgpr_write_b32 a55, v233\n"
    "v_accvgpr_read_b32 v232, a64\n"
    "v_mul_f32_e32 v232, v228, v232\n"
    "v_accvgpr_write_b32 a64, v232\n"
    "v_accvgpr_read_b32 v233, a65\n"
    "v_mul_f32_e32 v233, v229, v233\n"
    "v_accvgpr_write_b32 a65, v233\n"
    "v_accvgpr_read_b32 v232, a66\n"
    "v_mul_f32_e32 v232, v230, v232\n"
    "v_accvgpr_write_b32 a66, v232\n"
    "v_accvgpr_read_b32 v233, a67\n"
    "v_mul_f32_e32 v233, v231, v233\n"
    "v_accvgpr_write_b32 a67, v233\n"
    "v_accvgpr_read_b32 v232, a76\n"
    "v_mul_f32_e32 v232, v228, v232\n"
    "v_accvgpr_write_b32 a76, v232\n"
    "v_accvgpr_read_b32 v233, a77\n"
    "v_mul_f32_e32 v233, v229, v233\n"
    "v_accvgpr_write_b32 a77, v233\n"
    "v_accvgpr_read_b32 v232, a78\n"
    "v_mul_f32_e32 v232, v230, v232\n"
    "v_accvgpr_write_b32 a78, v232\n"
    "v_accvgpr_read_b32 v233, a79\n"
    "v_mul_f32_e32 v233, v231, v233\n"
    "v_accvgpr_write_b32 a79, v233\n"
    "v_accvgpr_read_b32 v232, a88\n"
    "v_mul_f32_e32 v232, v228, v232\n"
    "v_accvgpr_write_b32 a88, v232\n"
    "v_accvgpr_read_b32 v233, a89\n"
    "v_mul_f32_e32 v233, v229, v233\n"
    "v_accvgpr_write_b32 a89, v233\n"
    "v_accvgpr_read_b32 v232, a90\n"
    "v_mul_f32_e32 v232, v230, v232\n"
    "v_accvgpr_write_b32 a90, v232\n"
    "v_accvgpr_read_b32 v233, a91\n"
    "v_mul_f32_e32 v233, v231, v233\n"
    "v_accvgpr_write_b32 a91, v233\n"
    "v_accvgpr_read_b32 v232, a100\n"
    "v_mul_f32_e32 v232, v228, v232\n"
    "v_accvgpr_write_b32 a100, v232\n"
    "v_accvgpr_read_b32 v233, a101\n"
    "v_mul_f32_e32 v233, v229, v233\n"
    "v_accvgpr_write_b32 a101, v233\n"
    "v_accvgpr_read_b32 v232, a102\n"
    "v_mul_f32_e32 v232, v230, v232\n"
    "v_accvgpr_write_b32 a102, v232\n"
    "v_accvgpr_read_b32 v233, a103\n"
    "v_mul_f32_e32 v233, v231, v233\n"
    "v_accvgpr_write_b32 a103, v233\n"
    "v_accvgpr_read_b32 v232, a112\n"
    "v_mul_f32_e32 v232, v228, v232\n"
    "v_accvgpr_write_b32 a112, v232\n"
    "v_accvgpr_read_b32 v233, a113\n"
    "v_mul_f32_e32 v233, v229, v233\n"
    "v_accvgpr_write_b32 a113, v233\n"
    "v_accvgpr_read_b32 v232, a114\n"
    "v_mul_f32_e32 v232, v230, v232\n"
    "v_accvgpr_write_b32 a114, v232\n"
    "v_accvgpr_read_b32 v233, a115\n"
    "v_mul_f32_e32 v233, v231, v233\n"
    "v_accvgpr_write_b32 a115, v233\n"
    "v_accvgpr_read_b32 v232, a124\n"
    "v_mul_f32_e32 v232, v228, v232\n"
    "v_accvgpr_write_b32 a124, v232\n"
    "v_accvgpr_read_b32 v233, a125\n"
    "v_mul_f32_e32 v233, v229, v233\n"
    "v_accvgpr_write_b32 a125, v233\n"
    "v_accvgpr_read_b32 v232, a126\n"
    "v_mul_f32_e32 v232, v230, v232\n"
    "v_accvgpr_write_b32 a126, v232\n"
    "v_accvgpr_read_b32 v233, a127\n"
    "v_mul_f32_e32 v233, v231, v233\n"
    "v_accvgpr_write_b32 a127, v233\n"
    "v_accvgpr_read_b32 v232, a136\n"
    "v_mul_f32_e32 v232, v228, v232\n"
    "v_accvgpr_write_b32 a136, v232\n"
    "v_accvgpr_read_b32 v233, a137\n"
    "v_mul_f32_e32 v233, v229, v233\n"
    "v_accvgpr_write_b32 a137, v233\n"
    "v_accvgpr_read_b32 v232, a138\n"
    "v_mul_f32_e32 v232, v230, v232\n"
    "v_accvgpr_write_b32 a138, v232\n"
    "v_accvgpr_read_b32 v233, a139\n"
    "v_mul_f32_e32 v233, v231, v233\n"
    "v_accvgpr_write_b32 a139, v233\n"
    "v_accvgpr_read_b32 v232, a148\n"
    "v_mul_f32_e32 v232, v228, v232\n"
    "v_accvgpr_write_b32 a148, v232\n"
    "v_accvgpr_read_b32 v233, a149\n"
    "v_mul_f32_e32 v233, v229, v233\n"
    "v_accvgpr_write_b32 a149, v233\n"
    "v_accvgpr_read_b32 v232, a150\n"
    "v_mul_f32_e32 v232, v230, v232\n"
    "v_accvgpr_write_b32 a150, v232\n"
    "v_accvgpr_read_b32 v233, a151\n"
    "v_mul_f32_e32 v233, v231, v233\n"
    "v_accvgpr_write_b32 a151, v233\n"
    "v_accvgpr_read_b32 v232, a160\n"
    "v_mul_f32_e32 v232, v228, v232\n"
    "v_accvgpr_write_b32 a160, v232\n"
    "v_accvgpr_read_b32 v233, a161\n"
    "v_mul_f32_e32 v233, v229, v233\n"
    "v_accvgpr_write_b32 a161, v233\n"
    "v_accvgpr_read_b32 v232, a162\n"
    "v_mul_f32_e32 v232, v230, v232\n"
    "v_accvgpr_write_b32 a162, v232\n"
    "v_accvgpr_read_b32 v233, a163\n"
    "v_mul_f32_e32 v233, v231, v233\n"
    "v_accvgpr_write_b32 a163, v233\n"
    "v_accvgpr_read_b32 v232, a172\n"
    "v_mul_f32_e32 v232, v228, v232\n"
    "v_accvgpr_write_b32 a172, v232\n"
    "v_accvgpr_read_b32 v233, a173\n"
    "v_mul_f32_e32 v233, v229, v233\n"
    "v_accvgpr_write_b32 a173, v233\n"
    "v_accvgpr_read_b32 v232, a174\n"
    "v_mul_f32_e32 v232, v230, v232\n"
    "v_accvgpr_write_b32 a174, v232\n"
    "v_accvgpr_read_b32 v233, a175\n"
    "v_mul_f32_e32 v233, v231, v233\n"
    "v_accvgpr_write_b32 a175, v233\n"
    "v_accvgpr_read_b32 v232, a184\n"
    "v_mul_f32_e32 v232, v228, v232\n"
    "v_accvgpr_write_b32 a184, v232\n"
    "v_accvgpr_read_b32 v233, a185\n"
    "v_mul_f32_e32 v233, v229, v233\n"
    "v_accvgpr_write_b32 a185, v233\n"
    "v_accvgpr_read_b32 v232, a186\n"
    "v_mul_f32_e32 v232, v230, v232\n"
    "v_accvgpr_write_b32 a186, v232\n"
    "v_accvgpr_read_b32 v233, a187\n"
    "v_mul_f32_e32 v233, v231, v233\n"
    "v_accvgpr_write_b32 a187, v233\n"
    "v_max3_f32 v251, v56, v57, v58\n"
    "v_max3_f32 v251, v251, v59, v68\n"
    "v_max3_f32 v251, v251, v69, v70\n"
    "v_max3_f32 v251, v251, v71, v80\n"
    "v_max3_f32 v251, v251, v81, v82\n"
    "v_max3_f32 v251, v251, v83, v92\n"
    "v_max3_f32 v251, v251, v93, v94\n"
    "v_max_f32_e32 v251, v251, v95\n"
    "v_mov_b32_e32 v252, v251\n"
    "s_nop 1\n"
    "v_permlane16_swap_b32_e32 v252, v251\n"
    "s_nop 0\n"
    "v_max_f32_e32 v251, v251, v252\n"
    "v_mov_b32_e32 v252, v251\n"
    "s_nop 1\n"
    "v_permlane32_swap_b32_e32 v252, v251\n"
    "s_nop 0\n"
    "v_max_f32_e32 v251, v251, v252\n"
    "v_max_f32_e32 v251, 0, v251\n"
    "v_sub_f32_e32 v252, 0, v251\n"
    "v_exp_f32_e32 v252, v252\n"
    "s_nop 0\n"
    "v_mul_f32_e32 v239, v252, v239\n"
    "v_mbcnt_lo_u32_b32 v253, -1, 0\n"
    "v_mbcnt_hi_u32_b32 v253, -1, v253\n"
    "v_and_b32_e32 v253, 48, v253\n"
    "ds_bpermute_b32 v228, v253, v252 offset:0\n"
    "ds_bpermute_b32 v229, v253, v252 offset:4\n"
    "ds_bpermute_b32 v230, v253, v252 offset:8\n"
    "ds_bpermute_b32 v231, v253, v252 offset:12\n"
    "v_sub_f32_e32 v224, v224, v251\n"
    "v_sub_f32_e32 v225, v225, v251\n"
    "v_sub_f32_e32 v226, v226, v251\n"
    "v_sub_f32_e32 v227, v227, v251\n"
    "v_sub_f32_e32 v56, v56, v251\n"
    "v_exp_f32_e32 v56, v56\n"
    "v_sub_f32_e32 v57, v57, v251\n"
    "v_exp_f32_e32 v57, v57\n"
    "v_sub_f32_e32 v58, v58, v251\n"
    "v_exp_f32_e32 v58, v58\n"
    "v_sub_f32_e32 v59, v59, v251\n"
    "v_exp_f32_e32 v59, v59\n"
    "v_sub_f32_e32 v68, v68, v251\n"
    "v_exp_f32_e32 v68, v68\n"
    "v_sub_f32_e32 v69, v69, v251\n"
    "v_exp_f32_e32 v69, v69\n"
    "v_sub_f32_e32 v70, v70, v251\n"
    "v_exp_f32_e32 v70, v70\n"
    "v_sub_f32_e32 v71, v71, v251\n"
    "v_exp_f32_e32 v71, v71\n"
    "v_sub_f32_e32 v80, v80, v251\n"
    "v_exp_f32_e32 v80, v80\n"
    "v_sub_f32_e32 v81, v81, v251\n"
    "v_exp_f32_e32 v81, v81\n"
    "v_sub_f32_e32 v82, v82, v251\n"
    "v_exp_f32_e32 v82, v82\n"
    "v_sub_f32_e32 v83, v83, v251\n"
    "v_exp_f32_e32 v83, v83\n"
    "v_sub_f32_e32 v92, v92, v251\n"
    "v_exp_f32_e32 v92, v92\n"
    "v_sub_f32_e32 v93, v93, v251\n"
    "v_exp_f32_e32 v93, v93\n"
    "v_sub_f32_e32 v94, v94, v251\n"
    "v_exp_f32_e32 v94, v94\n"
    "v_sub_f32_e32 v95, v95, v251\n"
    "v_exp_f32_e32 v95, v95\n"
    "s_nop 0\n"
    "v_add_f32_e32 v236, v56, v57\n"
    "v_add_f32_e32 v236, v58, v236\n"
    "v_add_f32_e32 v236, v59, v236\n"
    "v_add_f32_e32 v236, v68, v236\n"
    "v_add_f32_e32 v236, v69, v236\n"
    "v_add_f32_e32 v236, v70, v236\n"
    "v_add_f32_e32 v236, v71, v236\n"
    "v_add_f32_e32 v236, v80, v236\n"
    "v_add_f32_e32 v236, v81, v236\n"
    "v_add_f32_e32 v236, v82, v236\n"
    "v_add_f32_e32 v236, v83, v236\n"
    "v_add_f32_e32 v236, v92, v236\n"
    "v_add_f32_e32 v236, v93, v236\n"
    "v_add_f32_e32 v236, v94, v236\n"
    "v_add_f32_e32 v236, v95, v236\n"
    "v_cvt_pk_bf16_f32 v136, v56, v57\n"
    "v_cvt_pk_bf16_f32 v137, v58, v59\n"
    "v_cvt_pk_bf16_f32 v138, v68, v69\n"
    "v_cvt_pk_bf16_f32 v139, v70, v71\n"
    "v_cvt_pk_bf16_f32 v140, v80, v81\n"
    "v_cvt_pk_bf16_f32 v141, v82, v83\n"
    "v_cvt_pk_bf16_f32 v142, v92, v93\n"
    "v_cvt_pk_bf16_f32 v143, v94, v95\n"
    "s_waitcnt lgkmcnt(0)\n"
    "v_accvgpr_read_b32 v232, a8\n"
    "v_mul_f32_e32 v232, v228, v232\n"
    "v_accvgpr_write_b32 a8, v232\n"
    "v_accvgpr_read_b32 v233, a9\n"
    "v_mul_f32_e32 v233, v229, v233\n"
    "v_accvgpr_write_b32 a9, v233\n"
    "v_accvgpr_read_b32 v232, a10\n"
    "v_mul_f32_e32 v232, v230, v232\n"
    "v_accvgpr_write_b32 a10, v232\n"
    "v_accvgpr_read_b32 v233, a11\n"
    "v_mul_f32_e32 v233, v231, v233\n"
    "v_accvgpr_write_b32 a11, v233\n"
    "v_accvgpr_read_b32 v232, a20\n"
    "v_mul_f32_e32 v232, v228, v232\n"
    "v_accvgpr_write_b32 a20, v232\n"
    "v_accvgpr_read_b32 v233, a21\n"
    "v_mul_f32_e32 v233, v229, v233\n"
    "v_accvgpr_write_b32 a21, v233\n"
    "v_accvgpr_read_b32 v232, a22\n"
    "v_mul_f32_e32 v232, v230, v232\n"
    "v_accvgpr_write_b32 a22, v232\n"
    "v_accvgpr_read_b32 v233, a23\n"
    "v_mul_f32_e32 v233, v231, v233\n"
    "v_accvgpr_write_b32 a23, v233\n"
    "v_accvgpr_read_b32 v232, a32\n"
    "v_mul_f32_e32 v232, v228, v232\n"
    "v_accvgpr_write_b32 a32, v232\n"
    "v_accvgpr_read_b32 v233, a33\n"
    "v_mul_f32_e32 v233, v229, v233\n"
    "v_accvgpr_write_b32 a33, v233\n"
    "v_accvgpr_read_b32 v232, a34\n"
    "v_mul_f32_e32 v232, v230, v232\n"
    "v_accvgpr_write_b32 a34, v232\n"
    "v_accvgpr_read_b32 v233, a35\n"
    "v_mul_f32_e32 v233, v231, v233\n"
    "v_accvgpr_write_b32 a35, v233\n"
    "v_accvgpr_read_b32 v232, a44\n"
    "v_mul_f32_e32 v232, v228, v232\n"
    "v_accvgpr_write_b32 a44, v232\n"
    "v_accvgpr_read_b32 v233, a45\n"
    "v_mul_f32_e32 v233, v229, v233\n"
    "v_accvgpr_write_b32 a45, v233\n"
    "v_accvgpr_read_b32 v232, a46\n"
    "v_mul_f32_e32 v232, v230, v232\n"
    "v_accvgpr_write_b32 a46, v232\n"
    "v_accvgpr_read_b32 v233, a47\n"
    "v_mul_f32_e32 v233, v231, v233\n"
    "v_accvgpr_write_b32 a47, v233\n"
    "v_accvgpr_read_b32 v232, a56\n"
    "v_mul_f32_e32 v232, v228, v232\n"
    "v_accvgpr_write_b32 a56, v232\n"
    "v_accvgpr_read_b32 v233, a57\n"
    "v_mul_f32_e32 v233, v229, v233\n"
    "v_accvgpr_write_b32 a57, v233\n"
    "v_accvgpr_read_b32 v232, a58\n"
    "v_mul_f32_e32 v232, v230, v232\n"
    "v_accvgpr_write_b32 a58, v232\n"
    "v_accvgpr_read_b32 v233, a59\n"
    "v_mul_f32_e32 v233, v231, v233\n"
    "v_accvgpr_write_b32 a59, v233\n"
    "v_accvgpr_read_b32 v232, a68\n"
    "v_mul_f32_e32 v232, v228, v232\n"
    "v_accvgpr_write_b32 a68, v232\n"
    "v_accvgpr_read_b32 v233, a69\n"
    "v_mul_f32_e32 v233, v229, v233\n"
    "v_accvgpr_write_b32 a69, v233\n"
    "v_accvgpr_read_b32 v232, a70\n"
    "v_mul_f32_e32 v232, v230, v232\n"
    "v_accvgpr_write_b32 a70, v232\n"
    "v_accvgpr_read_b32 v233, a71\n"
    "v_mul_f32_e32 v233, v231, v233\n"
    "v_accvgpr_write_b32 a71, v233\n"
    "v_accvgpr_read_b32 v232, a80\n"
    "v_mul_f32_e32 v232, v228, v232\n"
    "v_accvgpr_write_b32 a80, v232\n"
    "v_accvgpr_read_b32 v233, a81\n"
    "v_mul_f32_e32 v233, v229, v233\n"
    "v_accvgpr_write_b32 a81, v233\n"
    "v_accvgpr_read_b32 v232, a82\n"
    "v_mul_f32_e32 v232, v230, v232\n"
    "v_accvgpr_write_b32 a82, v232\n"
    "v_accvgpr_read_b32 v233, a83\n"
    "v_mul_f32_e32 v233, v231, v233\n"
    "v_accvgpr_write_b32 a83, v233\n"
    "v_accvgpr_read_b32 v232, a92\n"
    "v_mul_f32_e32 v232, v228, v232\n"
    "v_accvgpr_write_b32 a92, v232\n"
    "v_accvgpr_read_b32 v233, a93\n"
    "v_mul_f32_e32 v233, v229, v233\n"
    "v_accvgpr_write_b32 a93, v233\n"
    "v_accvgpr_read_b32 v232, a94\n"
    "v_mul_f32_e32 v232, v230, v232\n"
    "v_accvgpr_write_b32 a94, v232\n"
    "v_accvgpr_read_b32 v233, a95\n"
    "v_mul_f32_e32 v233, v231, v233\n"
    "v_accvgpr_write_b32 a95, v233\n"
    "v_accvgpr_read_b32 v232, a104\n"
    "v_mul_f32_e32 v232, v228, v232\n"
    "v_accvgpr_write_b32 a104, v232\n"
    "v_accvgpr_read_b32 v233, a105\n"
    "v_mul_f32_e32 v233, v229, v233\n"
    "v_accvgpr_write_b32 a105, v233\n"
    "v_accvgpr_read_b32 v232, a106\n"
    "v_mul_f32_e32 v232, v230, v232\n"
    "v_accvgpr_write_b32 a106, v232\n"
    "v_accvgpr_read_b32 v233, a107\n"
    "v_mul_f32_e32 v233, v231, v233\n"
    "v_accvgpr_write_b32 a107, v233\n"
    "v_accvgpr_read_b32 v232, a116\n"
    "v_mul_f32_e32 v232, v228, v232\n"
    "v_accvgpr_write_b32 a116, v232\n"
    "v_accvgpr_read_b32 v233, a117\n"
    "v_mul_f32_e32 v233, v229, v233\n"
    "v_accvgpr_write_b32 a117, v233\n"
    "v_accvgpr_read_b32 v232, a118\n"
    "v_mul_f32_e32 v232, v230, v232\n"
    "v_accvgpr_write_b32 a118, v232\n"
    "v_accvgpr_read_b32 v233, a119\n"
    "v_mul_f32_e32 v233, v231, v233\n"
    "v_accvgpr_write_b32 a119, v233\n"
    "v_accvgpr_read_b32 v232, a128\n"
    "v_mul_f32_e32 v232, v228, v232\n"
    "v_accvgpr_write_b32 a128, v232\n"
    "v_accvgpr_read_b32 v233, a129\n"
    "v_mul_f32_e32 v233, v229, v233\n"
    "v_accvgpr_write_b32 a129, v233\n"
    "v_accvgpr_read_b32 v232, a130\n"
    "v_mul_f32_e32 v232, v230, v232\n"
    "v_accvgpr_write_b32 a130, v232\n"
    "v_accvgpr_read_b32 v233, a131\n"
    "v_mul_f32_e32 v233, v231, v233\n"
    "v_accvgpr_write_b32 a131, v233\n"
    "v_accvgpr_read_b32 v232, a140\n"
    "v_mul_f32_e32 v232, v228, v232\n"
    "v_accvgpr_write_b32 a140, v232\n"
    "v_accvgpr_read_b32 v233, a141\n"
    "v_mul_f32_e32 v233, v229, v233\n"
    "v_accvgpr_write_b32 a141, v233\n"
    "v_accvgpr_read_b32 v232, a142\n"
    "v_mul_f32_e32 v232, v230, v232\n"
    "v_accvgpr_write_b32 a142, v232\n"
    "v_accvgpr_read_b32 v233, a143\n"
    "v_mul_f32_e32 v233, v231, v233\n"
    "v_accvgpr_write_b32 a143, v233\n"
    "v_accvgpr_read_b32 v232, a152\n"
    "v_mul_f32_e32 v232, v228, v232\n"
    "v_accvgpr_write_b32 a152, v232\n"
    "v_accvgpr_read_b32 v233, a153\n"
    "v_mul_f32_e32 v233, v229, v233\n"
    "v_accvgpr_write_b32 a153, v233\n"
    "v_accvgpr_read_b32 v232, a154\n"
    "v_mul_f32_e32 v232, v230, v232\n"
    "v_accvgpr_write_b32 a154, v232\n"
    "v_accvgpr_read_b32 v233, a155\n"
    "v_mul_f32_e32 v233, v231, v233\n"
    "v_accvgpr_write_b32 a155, v233\n"
    "v_accvgpr_read_b32 v232, a164\n"
    "v_mul_f32_e32 v232, v228, v232\n"
    "v_accvgpr_write_b32 a164, v232\n"
    "v_accvgpr_read_b32 v233, a165\n"
    "v_mul_f32_e32 v233, v229, v233\n"
    "v_accvgpr_write_b32 a165, v233\n"
    "v_accvgpr_read_b32 v232, a166\n"
    "v_mul_f32_e32 v232, v230, v232\n"
    "v_accvgpr_write_b32 a166, v232\n"
    "v_accvgpr_read_b32 v233, a167\n"
    "v_mul_f32_e32 v233, v231, v233\n"
    "v_accvgpr_write_b32 a167, v233\n"
    "v_accvgpr_read_b32 v232, a176\n"
    "v_mul_f32_e32 v232, v228, v232\n"
    "v_accvgpr_write_b32 a176, v232\n"
    "v_accvgpr_read_b32 v233, a177\n"
    "v_mul_f32_e32 v233, v229, v233\n"
    "v_accvgpr_write_b32 a177, v233\n"
    "v_accvgpr_read_b32 v232, a178\n"
    "v_mul_f32_e32 v232, v230, v232\n"
    "v_accvgpr_write_b32 a178, v232\n"
    "v_accvgpr_read_b32 v233, a179\n"
    "v_mul_f32_e32 v233, v231, v233\n"
    "v_accvgpr_write_b32 a179, v233\n"
    "v_accvgpr_read_b32 v232, a188\n"
    "v_mul_f32_e32 v232, v228, v232\n"
    "v_accvgpr_write_b32 a188, v232\n"
    "v_accvgpr_read_b32 v233, a189\n"
    "v_mul_f32_e32 v233, v229, v233\n"
    "v_accvgpr_write_b32 a189, v233\n"
    "v_accvgpr_read_b32 v232, a190\n"
    "v_mul_f32_e32 v232, v230, v232\n"
    "v_accvgpr_write_b32 a190, v232\n"
    "v_accvgpr_read_b32 v233, a191\n"
    "v_mul_f32_e32 v233, v231, v233\n"
    "v_accvgpr_write_b32 a191, v233\n"
    "s_nop 1\n"
    "s_branch RETT_%=\n"
    ".p2align 6\n"
    "TAIL_%=:\n"
    "v_mfma_f32_16x16x32_bf16 a[0:3], v[96:99], a[192:195], a[0:3]\n"
    "ds_read_b64_tr_b16 a[224:225], v241 offset:4096\n"
    "v_add_f32_e32 v239, v236, v239\n"
    "v_add_u32_e32 v247, -65536, v241\n"
    "v_mfma_f32_16x16x32_bf16 a[0:3], v[100:103], a[196:199], a[0:3]\n"
    "ds_read_b64_tr_b16 a[226:227], v241 offset:5120\n"
    "v_add_u32_e32 v248, -65536, v242\n"
    "v_add_f32_e32 v237, v234, v237\n"
    "v_mfma_f32_16x16x32_bf16 a[4:7], v[108:111], a[196:199], a[4:7]\n"
    "ds_read_b64_tr_b16 a[228:229], v241 offset:6144\n"
    "v_add_u32_e32 v249, -65536, v243\n"
    "v_add_u32_e32 v250, -65536, v244\n"
    "v_mfma_f32_16x16x32_bf16 a[4:7], v[104:107], a[192:195], a[4:7]\n"
    "ds_read_b64_tr_b16 a[230:231], v241 offset:7168\n"
    "v_add_f32_e32 v238, v235, v238\n"
    "v_mfma_f32_16x16x32_bf16 a[8:11], v[112:115], a[192:195], a[8:11]\n"
    "ds_read_b64_tr_b16 a[232:233], v242 offset:4096\n"
    "v_exp_f32_e32 v0, v48\n"
    "v_mfma_f32_16x16x32_bf16 a[8:11], v[116:119], a[196:199], a[8:11]\n"
    "s_waitcnt lgkmcnt(13)\n"
    "v_exp_f32_e32 v1, v49\n"
    "v_mfma_f32_16x16x32_bf16 a[20:23], v[116:119], a[204:207], a[20:23]\n"
    "ds_read_b64_tr_b16 a[234:235], v242 offset:5120\n"
    "v_exp_f32_e32 v2, v50\n"
    "v_mfma_f32_16x16x32_bf16 a[20:23], v[112:115], a[200:203], a[20:23]\n"
    "ds_read_b64_tr_b16 a[236:237], v242 offset:6144\n"
    "v_cvt_pk_bf16_f32 v120, v0, v1\n"
    "v_add_f32_e32 v234, v0, v1\n"
    "v_mfma_f32_16x16x32_bf16 a[16:19], v[104:107], a[200:203], a[16:19]\n"
    "ds_read_b64_tr_b16 a[238:239], v242 offset:7168\n"
    "v_exp_f32_e32 v3, v51\n"
    "v_mfma_f32_16x16x32_bf16 a[16:19], v[108:111], a[204:207], a[16:19]\n"
    "ds_read_b64_tr_b16 a[240:241], v243 offset:4096\n"
    "v_exp_f32_e32 v4, v52\n"
    "v_mfma_f32_16x16x32_bf16 a[12:15], v[100:103], a[204:207], a[12:15]\n"
    "ds_read_b64_tr_b16 a[242:243], v243 offset:5120\n"
    "v_add_f32_e32 v234, v2, v234\n"
    "v_cvt_pk_bf16_f32 v121, v2, v3\n"
    "v_mfma_f32_16x16x32_bf16 a[12:15], v[96:99], a[200:203], a[12:15]\n"
    "s_waitcnt lgkmcnt(10)\n"
    "ds_read_b64_tr_b16 a[244:245], v243 offset:6144\n"
    "v_add_f32_e32 v234, v3, v234\n"
    "v_mfma_f32_16x16x32_bf16 a[24:27], v[96:99], a[208:211], a[24:27]\n"
    "ds_read_b64_tr_b16 a[246:247], v243 offset:7168\n"
    "v_exp_f32_e32 v5, v53\n"
    "v_mfma_f32_16x16x32_bf16 a[24:27], v[100:103], a[212:215], a[24:27]\n"
    "ds_read_b64_tr_b16 a[248:249], v244 offset:4096\n"
    "v_add_f32_e32 v235, v4, v5\n"
    "v_cvt_pk_bf16_f32 v128, v4, v5\n"
    "v_mfma_f32_16x16x32_bf16 a[28:31], v[108:111], a[212:215], a[28:31]\n"
    "ds_read_b64_tr_b16 a[250:251], v244 offset:5120\n"
    "v_exp_f32_e32 v6, v54\n"
    "v_mfma_f32_16x16x32_bf16 a[28:31], v[104:107], a[208:211], a[28:31]\n"
    "ds_read_b64_tr_b16 a[252:253], v244 offset:6144\n"
    "v_exp_f32_e32 v7, v55\n"
    "v_mfma_f32_16x16x32_bf16 a[32:35], v[112:115], a[208:211], a[32:35]\n"
    "ds_read_b64_tr_b16 a[254:255], v244 offset:7168\n"
    "v_exp_f32_e32 v8, v56\n"
    "v_mfma_f32_16x16x32_bf16 a[32:35], v[116:119], a[212:215], a[32:35]\n"
    "v_exp_f32_e32 v9, v57\n"
    "v_add_f32_e32 v235, v6, v235\n"
    "v_mfma_f32_16x16x32_bf16 a[44:47], v[116:119], a[220:223], a[44:47]\n"
    "ds_read_b64_tr_b16 a[192:193], v241 offset:16384\n"
    "v_add_f32_e32 v235, v7, v235\n"
    "v_cvt_pk_bf16_f32 v129, v6, v7\n"
    "v_mfma_f32_16x16x32_bf16 a[44:47], v[112:115], a[216:219], a[44:47]\n"
    "ds_read_b64_tr_b16 a[194:195], v241 offset:17408\n"
    "v_exp_f32_e32 v10, v58\n"
    "v_mfma_f32_16x16x32_bf16 a[40:43], v[104:107], a[216:219], a[40:43]\n"
    "ds_read_b64_tr_b16 a[196:197], v241 offset:18432\n"
    "v_add_f32_e32 v236, v8, v9\n"
    "v_cvt_pk_bf16_f32 v136, v8, v9\n"
    "v_mfma_f32_16x16x32_bf16 a[40:43], v[108:111], a[220:223], a[40:43]\n"
    "ds_read_b64_tr_b16 a[198:199], v241 offset:19456\n"
    "v_exp_f32_e32 v11, v59\n"
    "v_mfma_f32_16x16x32_bf16 a[36:39], v[100:103], a[220:223], a[36:39]\n"
    "ds_read_b64_tr_b16 a[200:201], v242 offset:16384\n"
    "v_add_f32_e32 v236, v10, v236\n"
    "v_add_f32_e32 v236, v11, v236\n"
    "v_mfma_f32_16x16x32_bf16 a[36:39], v[96:99], a[216:219], a[36:39]\n"
    "s_waitcnt lgkmcnt(13)\n"
    "ds_read_b64_tr_b16 a[202:203], v242 offset:17408\n"
    "v_cvt_pk_bf16_f32 v137, v10, v11\n"
    "v_mfma_f32_16x16x32_bf16 a[48:51], v[96:99], a[224:227], a[48:51]\n"
    "ds_read_b64_tr_b16 a[204:205], v242 offset:18432\n"
    "v_exp_f32_e32 v12, v60\n"
    "v_mfma_f32_16x16x32_bf16 a[48:51], v[100:103], a[228:231], a[48:51]\n"
    "ds_read_b64_tr_b16 a[206:207], v242 offset:19456\n"
    "v_exp_f32_e32 v13, v61\n"
    "v_mfma_f32_16x16x32_bf16 a[52:55], v[108:111], a[228:231], a[52:55]\n"
    "ds_read_b64_tr_b16 a[208:209], v243 offset:16384\n"
    "v_add_f32_e32 v234, v12, v234\n"
    "v_add_f32_e32 v234, v13, v234\n"
    "v_mfma_f32_16x16x32_bf16 a[52:55], v[104:107], a[224:227], a[52:55]\n"
    "ds_read_b64_tr_b16 a[210:211], v243 offset:17408\n"
    "v_exp_f32_e32 v14, v62\n"
    "v_mfma_f32_16x16x32_bf16 a[56:59], v[112:115], a[224:227], a[56:59]\n"
    "v_exp_f32_e32 v15, v63\n"
    "v_cvt_pk_bf16_f32 v122, v12, v13\n"
    "v_mfma_f32_16x16x32_bf16 a[56:59], v[116:119], a[228:231], a[56:59]\n"
    "ds_read_b64_tr_b16 a[212:213], v243 offset:18432\n"
    "v_exp_f32_e32 v0, v64\n"
    "v_mfma_f32_16x16x32_bf16 a[68:71], v[116:119], a[236:239], a[68:71]\n"
    "ds_read_b64_tr_b16 a[214:215], v243 offset:19456\n"
    "v_add_f32_e32 v234, v14, v234\n"
    "v_cvt_pk_bf16_f32 v123, v14, v15\n"
    "v_mfma_f32_16x16x32_bf16 a[68:71], v[112:115], a[232:235], a[68:71]\n"
    "ds_read_b64_tr_b16 a[216:217], v244 offset:16384\n"
    "v_exp_f32_e32 v1, v65\n"
    "v_mfma_f32_16x16x32_bf16 a[64:67], v[104:107], a[232:235], a[64:67]\n"
    "ds_read_b64_tr_b16 a[218:219], v244 offset:17408\n"
    "v_add_f32_e32 v234, v15, v234\n"
    "v_add_f32_e32 v235, v0, v235\n"
    "v_mfma_f32_16x16x32_bf16 a[64:67], v[108:111], a[236:239], a[64:67]\n"
    "ds_read_b64_tr_b16 a[220:221], v244 offset:18432\n"
    "v_exp_f32_e32 v2, v66\n"
    "v_mfma_f32_16x16x32_bf16 a[60:63], v[100:103], a[236:239], a[60:63]\n"
    "ds_read_b64_tr_b16 a[222:223], v244 offset:19456\n"
    "v_cvt_pk_bf16_f32 v130, v0, v1\n"
    "v_add_f32_e32 v235, v1, v235\n"
    "v_mfma_f32_16x16x32_bf16 a[60:63], v[96:99], a[232:235], a[60:63]\n"
    "s_waitcnt lgkmcnt(15)\n"
    "v_exp_f32_e32 v3, v67\n"
    "v_mfma_f32_16x16x32_bf16 a[72:75], v[96:99], a[240:243], a[72:75]\n"
    "ds_read_b64_tr_b16 a[224:225], v241 offset:20480\n"
    "v_exp_f32_e32 v4, v68\n"
    "v_mfma_f32_16x16x32_bf16 a[72:75], v[100:103], a[244:247], a[72:75]\n"
    "ds_read_b64_tr_b16 a[226:227], v241 offset:21504\n"
    "v_add_f32_e32 v235, v2, v235\n"
    "v_cvt_pk_bf16_f32 v131, v2, v3\n"
    "v_mfma_f32_16x16x32_bf16 a[76:79], v[108:111], a[244:247], a[76:79]\n"
    "ds_read_b64_tr_b16 a[228:229], v241 offset:22528\n"
    "v_exp_f32_e32 v5, v69\n"
    "v_mfma_f32_16x16x32_bf16 a[76:79], v[104:107], a[240:243], a[76:79]\n"
    "ds_read_b64_tr_b16 a[230:231], v241 offset:23552\n"
    "v_add_f32_e32 v235, v3, v235\n"
    "v_add_f32_e32 v236, v4, v236\n"
    "v_mfma_f32_16x16x32_bf16 a[80:83], v[112:115], a[240:243], a[80:83]\n"
    "ds_read_b64_tr_b16 a[232:233], v242 offset:20480\n"
    "v_exp_f32_e32 v6, v70\n"
    "v_mfma_f32_16x16x32_bf16 a[80:83], v[116:119], a[244:247], a[80:83]\n"
    "ds_read_b64_tr_b16 a[234:235], v242 offset:21504\n"
    "v_add_f32_e32 v236, v5, v236\n"
    "v_cvt_pk_bf16_f32 v138, v4, v5\n"
    "v_mfma_f32_16x16x32_bf16 a[92:95], v[116:119], a[252:255], a[92:95]\n"
    "ds_read_b64_tr_b16 a[236:237], v242 offset:22528\n"
    "v_exp_f32_e32 v7, v71\n"
    "v_mfma_f32_16x16x32_bf16 a[92:95], v[112:115], a[248:251], a[92:95]\n"
    "ds_read_b64_tr_b16 a[238:239], v242 offset:23552\n"
    "v_add_f32_e32 v236, v6, v236\n"
    "v_add_f32_e32 v236, v7, v236\n"
    "v_mfma_f32_16x16x32_bf16 a[88:91], v[104:107], a[248:251], a[88:91]\n"
    "v_exp_f32_e32 v8, v72\n"
    "v_cvt_pk_bf16_f32 v139, v6, v7\n"
    "v_mfma_f32_16x16x32_bf16 a[88:91], v[108:111], a[252:255], a[88:91]\n"
    "ds_read_b64_tr_b16 a[240:241], v243 offset:20480\n"
    "v_exp_f32_e32 v9, v73\n"
    "v_mfma_f32_16x16x32_bf16 a[84:87], v[100:103], a[252:255], a[84:87]\n"
    "ds_read_b64_tr_b16 a[242:243], v243 offset:21504\n"
    "v_exp_f32_e32 v10, v74\n"
    "v_mfma_f32_16x16x32_bf16 a[84:87], v[96:99], a[248:251], a[84:87]\n"
    "s_waitcnt lgkmcnt(15)\n"
    "ds_read_b64_tr_b16 a[244:245], v243 offset:22528\n"
    "v_add_f32_e32 v234, v8, v234\n"
    "v_mfma_f32_16x16x32_bf16 a[96:99], v[96:99], a[192:195], a[96:99]\n"
    "ds_read_b64_tr_b16 a[246:247], v243 offset:23552\n"
    "v_cvt_pk_bf16_f32 v124, v8, v9\n"
    "v_add_f32_e32 v234, v9, v234\n"
    "v_mfma_f32_16x16x32_bf16 a[96:99], v[100:103], a[196:199], a[96:99]\n"
    "ds_read_b64_tr_b16 a[248:249], v244 offset:20480\n"
    "v_exp_f32_e32 v11, v75\n"
    "v_mfma_f32_16x16x32_bf16 a[100:103], v[108:111], a[196:199], a[100:103]\n"
    "ds_read_b64_tr_b16 a[250:251], v244 offset:21504\n"
    "v_exp_f32_e32 v12, v76\n"
    "v_mfma_f32_16x16x32_bf16 a[100:103], v[104:107], a[192:195], a[100:103]\n"
    "ds_read_b64_tr_b16 a[252:253], v244 offset:22528\n"
    "v_add_f32_e32 v234, v10, v234\n"
    "v_cvt_pk_bf16_f32 v125, v10, v11\n"
    "v_mfma_f32_16x16x32_bf16 a[104:107], v[112:115], a[192:195], a[104:107]\n"
    "v_exp_f32_e32 v13, v77\n"
    "v_add_f32_e32 v234, v11, v234\n"
    "v_mfma_f32_16x16x32_bf16 a[104:107], v[116:119], a[196:199], a[104:107]\n"
    "ds_read_b64_tr_b16 a[254:255], v244 offset:23552\n"
    "v_add_f32_e32 v235, v12, v235\n"
    "v_cvt_pk_bf16_f32 v132, v12, v13\n"
    "v_mfma_f32_16x16x32_bf16 a[116:119], v[116:119], a[204:207], a[116:119]\n"
    "v_exp_f32_e32 v14, v78\n"
    "v_add_f32_e32 v235, v13, v235\n"
    "v_mfma_f32_16x16x32_bf16 a[116:119], v[112:115], a[200:203], a[116:119]\n"
    "v_exp_f32_e32 v15, v79\n"
    "v_add_f32_e32 v235, v14, v235\n"
    "v_mfma_f32_16x16x32_bf16 a[112:115], v[104:107], a[200:203], a[112:115]\n"
    "s_waitcnt vmcnt(0)\n"
    "s_barrier\n"
    "v_add_f32_e32 v235, v15, v235\n"
    "v_mfma_f32_16x16x32_bf16 a[112:115], v[108:111], a[204:207], a[112:115]\n"
    "ds_read_b64_tr_b16 v[144:145], v247 offset:0\n"
    "v_exp_f32_e32 v0, v80\n"
    "v_mfma_f32_16x16x32_bf16 a[108:111], v[100:103], a[204:207], a[108:111]\n"
    "ds_read_b64_tr_b16 v[146:147], v247 offset:1024\n"
    "v_exp_f32_e32 v1, v81\n"
    "v_mfma_f32_16x16x32_bf16 a[108:111], v[96:99], a[200:203], a[108:111]\n"
    "s_waitcnt lgkmcnt(15)\n"
    "ds_read_b64_tr_b16 v[148:149], v247 offset:2048\n"
    "v_cvt_pk_bf16_f32 v133, v14, v15\n"
    "v_mfma_f32_16x16x32_bf16 a[120:123], v[96:99], a[208:211], a[120:123]\n"
    "ds_read_b64_tr_b16 v[150:151], v247 offset:3072\n"
    "v_exp_f32_e32 v2, v82\n"
    "v_mfma_f32_16x16x32_bf16 a[120:123], v[100:103], a[212:215], a[120:123]\n"
    "ds_read_b64_tr_b16 v[152:153], v248 offset:0\n"
    "v_add_f32_e32 v236, v0, v236\n"
    "v_add_f32_e32 v236, v1, v236\n"
    "v_mfma_f32_16x16x32_bf16 a[124:127], v[108:111], a[212:215], a[124:127]\n"
    "v_exp_f32_e32 v3, v83\n"
    "v_cvt_pk_bf16_f32 v140, v0, v1\n"
    "v_mfma_f32_16x16x32_bf16 a[124:127], v[104:107], a[208:211], a[124:127]\n"
    "ds_read_b64_tr_b16 v[154:155], v248 offset:1024\n"
    "v_add_f32_e32 v236, v2, v236\n"
    "v_cvt_pk_bf16_f32 v141, v2, v3\n"
    "v_mfma_f32_16x16x32_bf16 a[128:131], v[112:115], a[208:211], a[128:131]\n"
    "ds_read_b64_tr_b16 v[156:157], v248 offset:2048\n"
    "v_exp_f32_e32 v4, v84\n"
    "v_mfma_f32_16x16x32_bf16 a[128:131], v[116:119], a[212:215], a[128:131]\n"
    "ds_read_b64_tr_b16 v[158:159], v248 offset:3072\n"
    "v_add_f32_e32 v236, v3, v236\n"
    "v_add_f32_e32 v234, v4, v234\n"
    "v_mfma_f32_16x16x32_bf16 a[140:143], v[116:119], a[220:223], a[140:143]\n"
    "ds_read_b64_tr_b16 v[160:161], v249 offset:0\n"
    "v_exp_f32_e32 v5, v85\n"
    "v_mfma_f32_16x16x32_bf16 a[140:143], v[112:115], a[216:219], a[140:143]\n"
    "ds_read_b64_tr_b16 v[162:163], v249 offset:1024\n"
    "v_exp_f32_e32 v6, v86\n"
    "v_mfma_f32_16x16x32_bf16 a[136:139], v[104:107], a[216:219], a[136:139]\n"
    "ds_read_b64_tr_b16 v[164:165], v249 offset:2048\n"
    "v_add_f32_e32 v234, v5, v234\n"
    "v_cvt_pk_bf16_f32 v126, v4, v5\n"
    "v_mfma_f32_16x16x32_bf16 a[136:139], v[108:111], a[220:223], a[136:139]\n"
    "ds_read_b64_tr_b16 v[166:167], v249 offset:3072\n"
    "v_exp_f32_e32 v7, v87\n"
    "v_mfma_f32_16x16x32_bf16 a[132:135], v[100:103], a[220:223], a[132:135]\n"
    "ds_read_b64_tr_b16 v[168:169], v250 offset:0\n"
    "v_exp_f32_e32 v8, v88\n"
    "v_mfma_f32_16x16x32_bf16 a[132:135], v[96:99], a[216:219], a[132:135]\n"
    "s_waitcnt lgkmcnt(15)\n"
    "ds_read_b64_tr_b16 v[170:171], v250 offset:1024\n"
    "v_add_f32_e32 v234, v6, v234\n"
    "v_mfma_f32_16x16x32_bf16 a[144:147], v[96:99], a[224:227], a[144:147]\n"
    "v_exp_f32_e32 v9, v89\n"
    "v_cvt_pk_bf16_f32 v127, v6, v7\n"
    "v_mfma_f32_16x16x32_bf16 a[144:147], v[100:103], a[228:231], a[144:147]\n"
    "ds_read_b64_tr_b16 v[172:173], v250 offset:2048\n"
    "v_add_f32_e32 v234, v7, v234\n"
    "v_add_f32_e32 v235, v8, v235\n"
    "v_mfma_f32_16x16x32_bf16 a[148:151], v[108:111], a[228:231], a[148:151]\n"
    "ds_read_b64_tr_b16 v[174:175], v250 offset:3072\n"
    "v_add_f32_e32 v235, v9, v235\n"
    "v_cvt_pk_bf16_f32 v134, v8, v9\n"
    "v_mfma_f32_16x16x32_bf16 a[148:151], v[104:107], a[224:227], a[148:151]\n"
    "ds_read_b64_tr_b16 v[176:177], v247 offset:4096\n"
    "v_exp_f32_e32 v10, v90\n"
    "v_mfma_f32_16x16x32_bf16 a[152:155], v[112:115], a[224:227], a[152:155]\n"
    "ds_read_b64_tr_b16 v[178:179], v247 offset:5120\n"
    "v_exp_f32_e32 v11, v91\n"
    "v_mfma_f32_16x16x32_bf16 a[152:155], v[116:119], a[228:231], a[152:155]\n"
    "ds_read_b64_tr_b16 v[180:181], v247 offset:6144\n"
    "v_add_f32_e32 v235, v10, v235\n"
    "v_add_f32_e32 v235, v11, v235\n"
    "v_mfma_f32_16x16x32_bf16 a[164:167], v[116:119], a[236:239], a[164:167]\n"
    "ds_read_b64_tr_b16 v[182:183], v247 offset:7168\n"
    "v_exp_f32_e32 v12, v92\n"
    "v_mfma_f32_16x16x32_bf16 a[164:167], v[112:115], a[232:235], a[164:167]\n"
    "ds_read_b64_tr_b16 v[184:185], v248 offset:4096\n"
    "v_cvt_pk_bf16_f32 v135, v10, v11\n"
    "v_add_f32_e32 v236, v12, v236\n"
    "v_mfma_f32_16x16x32_bf16 a[160:163], v[104:107], a[232:235], a[160:163]\n"
    "ds_read_b64_tr_b16 v[186:187], v248 offset:5120\n"
    "v_exp_f32_e32 v13, v93\n"
    "v_mfma_f32_16x16x32_bf16 a[160:163], v[108:111], a[236:239], a[160:163]\n"
    "ds_read_b64_tr_b16 v[188:189], v248 offset:6144\n"
    "v_exp_f32_e32 v14, v94\n"
    "v_mfma_f32_16x16x32_bf16 a[156:159], v[100:103], a[236:239], a[156:159]\n"
    "v_exp_f32_e32 v15, v95\n"
    "v_cvt_pk_bf16_f32 v142, v12, v13\n"
    "v_mfma_f32_16x16x32_bf16 a[156:159], v[96:99], a[232:235], a[156:159]\n"
    "s_waitcnt lgkmcnt(15)\n"
    "ds_read_b64_tr_b16 v[190:191], v248 offset:7168\n"
    "v_add_f32_e32 v236, v13, v236\n"
    "v_mfma_f32_16x16x32_bf16 a[168:171], v[96:99], a[240:243], a[168:171]\n"
    "ds_read_b64_tr_b16 v[192:193], v249 offset:4096\n"
    "v_add_f32_e32 v236, v14, v236\n"
    "v_cvt_pk_bf16_f32 v143, v14, v15\n"
    "v_mfma_f32_16x16x32_bf16 a[168:171], v[100:103], a[244:247], a[168:171]\n"
    "ds_read_b64_tr_b16 v[194:195], v249 offset:5120\n"
    "v_add_f32_e32 v236, v15, v236\n"
    "v_max3_f32 v251, v234, v235, v236\n"
    "v_mfma_f32_16x16x32_bf16 a[172:175], v[108:111], a[244:247], a[172:175]\n"
    "ds_read_b64_tr_b16 v[196:197], v249 offset:6144\n"
    "v_cmp_lt_f32_e32 vcc, s57, v251\n"
    "v_mfma_f32_16x16x32_bf16 a[172:175], v[104:107], a[240:243], a[172:175]\n"
    "ds_read_b64_tr_b16 v[198:199], v249 offset:7168\n"
    "v_mfma_f32_16x16x32_bf16 a[176:179], v[112:115], a[240:243], a[176:179]\n"
    "ds_read_b64_tr_b16 v[200:201], v250 offset:4096\n"
    "v_mfma_f32_16x16x32_bf16 a[176:179], v[116:119], a[244:247], a[176:179]\n"
    "ds_read_b64_tr_b16 v[202:203], v250 offset:5120\n"
    "v_mfma_f32_16x16x32_bf16 a[188:191], v[116:119], a[252:255], a[188:191]\n"
    "ds_read_b64_tr_b16 v[204:205], v250 offset:6144\n"
    "v_mfma_f32_16x16x32_bf16 a[188:191], v[112:115], a[248:251], a[188:191]\n"
    "ds_read_b64_tr_b16 v[206:207], v250 offset:7168\n"
    "v_mfma_f32_16x16x32_bf16 a[184:187], v[104:107], a[248:251], a[184:187]\n"
    "v_mfma_f32_16x16x32_bf16 a[184:187], v[108:111], a[252:255], a[184:187]\n"
    "v_mfma_f32_16x16x32_bf16 a[180:183], v[100:103], a[252:255], a[180:183]\n"
    "v_mfma_f32_16x16x32_bf16 a[180:183], v[96:99], a[248:251], a[180:183]\n"
    "s_cbranch_vccnz RARET_%=\n"
    "RETT_%=:\n"
    "s_waitcnt lgkmcnt(0)\n"
    "v_mfma_f32_16x16x32_bf16 a[0:3], v[120:123], v[144:147], a[0:3]\n"
    "v_add_f32_e32 v237, v234, v237\n"
    "v_mov_b32_e32 v112, v237\n"
    "v_add_f32_e32 v238, v235, v238\n"
    "v_mov_b32_e32 v113, v238\n"
    "v_mfma_f32_16x16x32_bf16 a[0:3], v[124:127], v[148:151], a[0:3]\n"
    "v_add_f32_e32 v239, v236, v239\n"
    "v_mov_b32_e32 v114, v239\n"
    "v_mbcnt_lo_u32_b32 v0, -1, 0\n"
    "v_mbcnt_hi_u32_b32 v0, -1, v0\n"
    "v_mfma_f32_16x16x32_bf16 a[12:15], v[124:127], v[156:159], a[12:15]\n"
    "s_lshl_b32 s58, s85, 14\n"
    "v_permlane16_swap_b32_e32 v112, v237\n"
    "v_permlane16_swap_b32_e32 v113, v238\n"
    "v_and_b32_e32 v1, 48, v0\n"
    "v_mfma_f32_16x16x32_bf16 a[12:15], v[120:123], v[152:155], a[12:15]\n"
    "v_add_f32_e32 v237, v237, v112\n"
    "v_mov_b32_e32 v112, v237\n"
    "v_add_f32_e32 v238, v238, v113\n"
    "v_mov_b32_e32 v113, v238\n"
    "v_mfma_f32_16x16x32_bf16 a[24:27], v[120:123], v[160:163], a[24:27]\n"
    "s_add_u32 s58, s58, 98304\n"
    "v_permlane16_swap_b32_e32 v114, v239\n"
    "v_and_b32_e32 v2, 15, v0\n"
    "v_lshlrev_b32_e32 v2, 4, v2\n"
    "v_mfma_f32_16x16x32_bf16 a[24:27], v[124:127], v[164:167], a[24:27]\n"
    "v_permlane32_swap_b32_e32 v112, v237\n"
    "v_add_f32_e32 v239, v239, v114\n"
    "v_permlane32_swap_b32_e32 v113, v238\n"
    "v_mov_b32_e32 v114, v239\n"
    "v_mfma_f32_16x16x32_bf16 a[36:39], v[124:127], v[172:175], a[36:39]\n"
    "v_add_f32_e32 v237, v237, v112\n"
    "v_rcp_f32_e32 v237, v237\n"
    "v_add_f32_e32 v238, v238, v113\n"
    "v_mfma_f32_16x16x32_bf16 a[36:39], v[120:123], v[168:171], a[36:39]\n"
    "ds_bpermute_b32 v216, v1, v237 offset:0\n"
    "ds_bpermute_b32 v217, v1, v237 offset:4\n"
    "v_rcp_f32_e32 v238, v238\n"
    "v_mfma_f32_16x16x32_bf16 a[48:51], v[120:123], v[176:179], a[48:51]\n"
    "ds_bpermute_b32 v218, v1, v237 offset:8\n"
    "ds_bpermute_b32 v219, v1, v237 offset:12\n"
    "v_permlane32_swap_b32_e32 v114, v239\n"
    "v_lshl_add_u32 v246, v1, 5, v2\n"
    "v_mfma_f32_16x16x32_bf16 a[48:51], v[124:127], v[180:183], a[48:51]\n"
    "ds_bpermute_b32 v220, v1, v238 offset:0\n"
    "ds_bpermute_b32 v221, v1, v238 offset:4\n"
    "v_lshl_add_u32 v2, v1, 6, v2\n"
    "v_add_f32_e32 v239, v239, v114\n"
    "v_mfma_f32_16x16x32_bf16 a[60:63], v[124:127], v[188:191], a[60:63]\n"
    "ds_bpermute_b32 v222, v1, v238 offset:8\n"
    "ds_bpermute_b32 v223, v1, v238 offset:12\n"
    "v_add_u32_e32 v232, s58, v2\n"
    "v_lshlrev_b32_e32 v3, 4, v1\n"
    "v_mfma_f32_16x16x32_bf16 a[60:63], v[120:123], v[184:187], a[60:63]\n"
    "v_and_b32_e32 v2, 1, v0\n"
    "v_rcp_f32_e32 v239, v239\n"
    "v_lshl_add_u32 v3, v2, 7, v3\n"
    "v_mfma_f32_16x16x32_bf16 a[72:75], v[120:123], v[192:195], a[72:75]\n"
    "ds_bpermute_b32 v224, v1, v239 offset:0\n"
    "ds_bpermute_b32 v225, v1, v239 offset:4\n"
    "v_bfe_u32 v2, v0, 2, 2\n"
    "v_lshl_add_u32 v3, v2, 4, v3\n"
    "v_mfma_f32_16x16x32_bf16 a[72:75], v[124:127], v[196:199], a[72:75]\n"
    "ds_bpermute_b32 v226, v1, v239 offset:8\n"
    "ds_bpermute_b32 v227, v1, v239 offset:12\n"
    "v_bfe_u32 v2, v0, 1, 1\n"
    "v_lshl_add_u32 v3, v2, 3, v3\n"
    "v_mfma_f32_16x16x32_bf16 a[84:87], v[124:127], v[204:207], a[84:87]\n"
    "v_add_u32_e32 v233, s58, v3\n"
    "v_mfma_f32_16x16x32_bf16 a[84:87], v[120:123], v[200:203], a[84:87]\n"
    "v_mfma_f32_16x16x32_bf16 a[4:7], v[128:131], v[144:147], a[4:7]\n"
    "s_waitcnt lgkmcnt(0)\n"
    "s_nop 3\n"
    "v_accvgpr_read_b32 v48, a0\n"
    "v_accvgpr_read_b32 v49, a1\n"
    "v_accvgpr_read_b32 v50, a2\n"
    "v_accvgpr_read_b32 v51, a3\n"
    "v_mfma_f32_16x16x32_bf16 a[4:7], v[132:135], v[148:151], a[4:7]\n"
    "ds_read_b64_tr_b16 a[192:193], v247 offset:16384\n"
    "ds_read_b64_tr_b16 a[194:195], v247 offset:17408\n"
    "v_accvgpr_read_b32 v52, a12\n"
    "v_accvgpr_read_b32 v53, a13\n"
    "v_accvgpr_read_b32 v54, a14\n"
    "v_accvgpr_read_b32 v55, a15\n"
    "v_accvgpr_read_b32 v56, a24\n"
    "v_mfma_f32_16x16x32_bf16 a[16:19], v[132:135], v[156:159], a[16:19]\n"
    "ds_read_b64_tr_b16 a[196:197], v247 offset:18432\n"
    "ds_read_b64_tr_b16 a[198:199], v247 offset:19456\n"
    "v_accvgpr_read_b32 v57, a25\n"
    "v_accvgpr_read_b32 v58, a26\n"
    "v_accvgpr_read_b32 v59, a27\n"
    "v_accvgpr_read_b32 v60, a36\n"
    "v_accvgpr_read_b32 v61, a37\n"
    "v_mfma_f32_16x16x32_bf16 a[16:19], v[128:131], v[152:155], a[16:19]\n"
    "ds_read_b64_tr_b16 a[200:201], v248 offset:16384\n"
    "ds_read_b64_tr_b16 a[202:203], v248 offset:17408\n"
    "v_accvgpr_read_b32 v62, a38\n"
    "v_accvgpr_read_b32 v63, a39\n"
    "v_mul_f32_e32 v48, v216, v48\n"
    "v_mul_f32_e32 v49, v217, v49\n"
    "v_mul_f32_e32 v50, v218, v50\n"
    "v_mfma_f32_16x16x32_bf16 a[28:31], v[128:131], v[160:163], a[28:31]\n"
    "ds_read_b64_tr_b16 a[204:205], v248 offset:18432\n"
    "ds_read_b64_tr_b16 a[206:207], v248 offset:19456\n"
    "v_mul_f32_e32 v51, v219, v51\n"
    "v_mul_f32_e32 v52, v216, v52\n"
    "v_mul_f32_e32 v53, v217, v53\n"
    "v_mul_f32_e32 v54, v218, v54\n"
    "v_mul_f32_e32 v55, v219, v55\n"
    "v_mfma_f32_16x16x32_bf16 a[28:31], v[132:135], v[164:167], a[28:31]\n"
    "ds_read_b64_tr_b16 a[208:209], v249 offset:16384\n"
    "ds_read_b64_tr_b16 a[210:211], v249 offset:17408\n"
    "v_mul_f32_e32 v56, v216, v56\n"
    "v_mul_f32_e32 v57, v217, v57\n"
    "v_mul_f32_e32 v58, v218, v58\n"
    "v_mul_f32_e32 v59, v219, v59\n"
    "v_mul_f32_e32 v60, v216, v60\n"
    "v_mfma_f32_16x16x32_bf16 a[40:43], v[132:135], v[172:175], a[40:43]\n"
    "ds_read_b64_tr_b16 a[212:213], v249 offset:18432\n"
    "ds_read_b64_tr_b16 a[214:215], v249 offset:19456\n"
    "v_mul_f32_e32 v61, v217, v61\n"
    "v_mul_f32_e32 v62, v218, v62\n"
    "v_mul_f32_e32 v63, v219, v63\n"
    "v_cvt_pk_bf16_f32 v80, v48, v52\n"
    "v_cvt_pk_bf16_f32 v81, v56, v60\n"
    "v_mfma_f32_16x16x32_bf16 a[40:43], v[128:131], v[168:171], a[40:43]\n"
    "ds_read_b64_tr_b16 a[216:217], v250 offset:16384\n"
    "ds_read_b64_tr_b16 a[218:219], v250 offset:17408\n"
    "v_cvt_pk_bf16_f32 v84, v49, v53\n"
    "v_cvt_pk_bf16_f32 v85, v57, v61\n"
    "v_cvt_pk_bf16_f32 v88, v50, v54\n"
    "v_cvt_pk_bf16_f32 v89, v58, v62\n"
    "v_cvt_pk_bf16_f32 v92, v51, v55\n"
    "v_mfma_f32_16x16x32_bf16 a[52:55], v[128:131], v[176:179], a[52:55]\n"
    "ds_read_b64_tr_b16 a[220:221], v250 offset:18432\n"
    "ds_read_b64_tr_b16 a[222:223], v250 offset:19456\n"
    "v_cvt_pk_bf16_f32 v93, v59, v63\n"
    "v_accvgpr_read_b32 v64, a48\n"
    "v_accvgpr_read_b32 v65, a49\n"
    "v_accvgpr_read_b32 v66, a50\n"
    "v_accvgpr_read_b32 v67, a51\n"
    "v_mfma_f32_16x16x32_bf16 a[52:55], v[132:135], v[180:183], a[52:55]\n"
    "ds_read_b64_tr_b16 a[224:225], v247 offset:20480\n"
    "ds_read_b64_tr_b16 a[226:227], v247 offset:21504\n"
    "v_accvgpr_read_b32 v68, a60\n"
    "v_accvgpr_read_b32 v69, a61\n"
    "v_accvgpr_read_b32 v70, a62\n"
    "v_accvgpr_read_b32 v71, a63\n"
    "v_accvgpr_read_b32 v72, a72\n"
    "v_mfma_f32_16x16x32_bf16 a[64:67], v[132:135], v[188:191], a[64:67]\n"
    "ds_read_b64_tr_b16 a[228:229], v247 offset:22528\n"
    "ds_read_b64_tr_b16 a[230:231], v247 offset:23552\n"
    "v_accvgpr_read_b32 v73, a73\n"
    "v_accvgpr_read_b32 v74, a74\n"
    "v_accvgpr_read_b32 v75, a75\n"
    "v_accvgpr_read_b32 v76, a84\n"
    "v_accvgpr_read_b32 v77, a85\n"
    "v_mfma_f32_16x16x32_bf16 a[64:67], v[128:131], v[184:187], a[64:67]\n"
    "ds_read_b64_tr_b16 a[232:233], v248 offset:20480\n"
    "ds_read_b64_tr_b16 a[234:235], v248 offset:21504\n"
    "v_accvgpr_read_b32 v78, a86\n"
    "v_accvgpr_read_b32 v79, a87\n"
    "v_mul_f32_e32 v64, v216, v64\n"
    "v_mul_f32_e32 v65, v217, v65\n"
    "v_mul_f32_e32 v66, v218, v66\n"
    "v_mfma_f32_16x16x32_bf16 a[76:79], v[128:131], v[192:195], a[76:79]\n"
    "ds_read_b64_tr_b16 a[236:237], v248 offset:22528\n"
    "ds_read_b64_tr_b16 a[238:239], v248 offset:23552\n"
    "v_mul_f32_e32 v67, v219, v67\n"
    "v_mul_f32_e32 v68, v216, v68\n"
    "v_mul_f32_e32 v69, v217, v69\n"
    "v_mul_f32_e32 v70, v218, v70\n"
    "v_mul_f32_e32 v71, v219, v71\n"
    "v_mfma_f32_16x16x32_bf16 a[76:79], v[132:135], v[196:199], a[76:79]\n"
    "ds_read_b64_tr_b16 a[240:241], v249 offset:20480\n"
    "ds_read_b64_tr_b16 a[242:243], v249 offset:21504\n"
    "v_mul_f32_e32 v72, v216, v72\n"
    "v_mul_f32_e32 v73, v217, v73\n"
    "v_mul_f32_e32 v74, v218, v74\n"
    "v_mul_f32_e32 v75, v219, v75\n"
    "v_mul_f32_e32 v76, v216, v76\n"
    "v_mfma_f32_16x16x32_bf16 a[88:91], v[132:135], v[204:207], a[88:91]\n"
    "v_mul_f32_e32 v77, v217, v77\n"
    "v_mul_f32_e32 v78, v218, v78\n"
    "v_mul_f32_e32 v79, v219, v79\n"
    "v_cvt_pk_bf16_f32 v82, v64, v68\n"
    "v_cvt_pk_bf16_f32 v83, v72, v76\n"
    "v_mfma_f32_16x16x32_bf16 a[88:91], v[128:131], v[200:203], a[88:91]\n"
    "v_cvt_pk_bf16_f32 v86, v65, v69\n"
    "v_cvt_pk_bf16_f32 v87, v73, v77\n"
    "v_cvt_pk_bf16_f32 v90, v66, v70\n"
    "v_cvt_pk_bf16_f32 v91, v74, v78\n"
    "v_cvt_pk_bf16_f32 v94, v67, v71\n"
    "v_cvt_pk_bf16_f32 v95, v75, v79\n"
    "v_mfma_f32_16x16x32_bf16 a[8:11], v[136:139], v[144:147], a[8:11]\n"
    "ds_write_b128 v232, v[80:83] offset:0\n"
    "ds_write_b128 v232, v[84:87] offset:256\n"
    "v_accvgpr_read_b32 v48, a4\n"
    "v_accvgpr_read_b32 v49, a5\n"
    "v_accvgpr_read_b32 v50, a6\n"
    "v_accvgpr_read_b32 v51, a7\n"
    "v_accvgpr_read_b32 v52, a16\n"
    "v_mfma_f32_16x16x32_bf16 a[8:11], v[140:143], v[148:151], a[8:11]\n"
    "ds_write_b128 v232, v[88:91] offset:512\n"
    "ds_write_b128 v232, v[92:95] offset:768\n"
    "v_accvgpr_read_b32 v53, a17\n"
    "v_accvgpr_read_b32 v54, a18\n"
    "v_accvgpr_read_b32 v55, a19\n"
    "v_accvgpr_read_b32 v56, a28\n"
    "v_accvgpr_read_b32 v57, a29\n"
    "v_mfma_f32_16x16x32_bf16 a[20:23], v[140:143], v[156:159], a[20:23]\n"
    "v_accvgpr_read_b32 v58, a30\n"
    "v_accvgpr_read_b32 v59, a31\n"
    "v_accvgpr_read_b32 v60, a40\n"
    "v_accvgpr_read_b32 v61, a41\n"
    "v_accvgpr_read_b32 v62, a42\n"
    "v_mfma_f32_16x16x32_bf16 a[20:23], v[136:139], v[152:155], a[20:23]\n"
    "v_accvgpr_read_b32 v63, a43\n"
    "v_mul_f32_e32 v48, v220, v48\n"
    "v_mul_f32_e32 v49, v221, v49\n"
    "v_mul_f32_e32 v50, v222, v50\n"
    "v_mul_f32_e32 v51, v223, v51\n"
    "v_mfma_f32_16x16x32_bf16 a[32:35], v[136:139], v[160:163], a[32:35]\n"
    "s_waitcnt lgkmcnt(0)\n"
    "ds_read_b64_tr_b16 v[0:1], v233 offset:0\n"
    "ds_read_b64_tr_b16 v[2:3], v233 offset:64\n"
    "v_mul_f32_e32 v52, v220, v52\n"
    "v_mul_f32_e32 v53, v221, v53\n"
    "v_mul_f32_e32 v54, v222, v54\n"
    "v_mul_f32_e32 v55, v223, v55\n"
    "v_mul_f32_e32 v56, v220, v56\n"
    "v_mfma_f32_16x16x32_bf16 a[32:35], v[140:143], v[164:167], a[32:35]\n"
    "ds_read_b64_tr_b16 v[4:5], v233 offset:1024\n"
    "ds_read_b64_tr_b16 v[6:7], v233 offset:1088\n"
    "v_mul_f32_e32 v57, v221, v57\n"
    "v_mul_f32_e32 v58, v222, v58\n"
    "v_mul_f32_e32 v59, v223, v59\n"
    "v_mul_f32_e32 v60, v220, v60\n"
    "v_mul_f32_e32 v61, v221, v61\n"
    "v_mfma_f32_16x16x32_bf16 a[44:47], v[140:143], v[172:175], a[44:47]\n"
    "ds_read_b64_tr_b16 v[8:9], v233 offset:2048\n"
    "ds_read_b64_tr_b16 v[10:11], v233 offset:2112\n"
    "v_mul_f32_e32 v62, v222, v62\n"
    "v_mul_f32_e32 v63, v223, v63\n"
    "v_cvt_pk_bf16_f32 v96, v48, v52\n"
    "v_cvt_pk_bf16_f32 v97, v56, v60\n"
    "v_cvt_pk_bf16_f32 v100, v49, v53\n"
    "v_mfma_f32_16x16x32_bf16 a[44:47], v[136:139], v[168:171], a[44:47]\n"
    "ds_read_b64_tr_b16 v[12:13], v233 offset:3072\n"
    "ds_read_b64_tr_b16 v[14:15], v233 offset:3136\n"
    "v_cvt_pk_bf16_f32 v101, v57, v61\n"
    "v_cvt_pk_bf16_f32 v104, v50, v54\n"
    "v_cvt_pk_bf16_f32 v105, v58, v62\n"
    "v_cvt_pk_bf16_f32 v108, v51, v55\n"
    "v_cvt_pk_bf16_f32 v109, v59, v63\n"
    "v_mfma_f32_16x16x32_bf16 a[56:59], v[136:139], v[176:179], a[56:59]\n"
    "ds_read_b64_tr_b16 a[244:245], v249 offset:22528\n"
    "ds_read_b64_tr_b16 a[246:247], v249 offset:23552\n"
    "v_accvgpr_read_b32 v64, a52\n"
    "v_accvgpr_read_b32 v65, a53\n"
    "v_accvgpr_read_b32 v66, a54\n"
    "v_accvgpr_read_b32 v67, a55\n"
    "v_accvgpr_read_b32 v68, a64\n"
    "v_mfma_f32_16x16x32_bf16 a[56:59], v[140:143], v[180:183], a[56:59]\n"
    "ds_read_b64_tr_b16 a[248:249], v250 offset:20480\n"
    "ds_read_b64_tr_b16 a[250:251], v250 offset:21504\n"
    "v_accvgpr_read_b32 v69, a65\n"
    "v_accvgpr_read_b32 v70, a66\n"
    "v_accvgpr_read_b32 v71, a67\n"
    "v_accvgpr_read_b32 v72, a76\n"
    "v_accvgpr_read_b32 v73, a77\n"
    "v_mfma_f32_16x16x32_bf16 a[68:71], v[140:143], v[188:191], a[68:71]\n"
    "ds_read_b64_tr_b16 a[252:253], v250 offset:22528\n"
    "ds_read_b64_tr_b16 a[254:255], v250 offset:23552\n"
    "v_accvgpr_read_b32 v74, a78\n"
    "v_accvgpr_read_b32 v75, a79\n"
    "v_accvgpr_read_b32 v76, a88\n"
    "v_accvgpr_read_b32 v77, a89\n"
    "v_accvgpr_read_b32 v78, a90\n"
    "v_mfma_f32_16x16x32_bf16 a[68:71], v[136:139], v[184:187], a[68:71]\n"
    "s_waitcnt lgkmcnt(6)\n"
    "s_mov_b32 s59, 0\n"
    "buffer_store_dwordx4 v[0:3], v246, s[48:51], s59 offen offset:0 nt\n"
    "v_accvgpr_read_b32 v79, a91\n"
    "v_mul_f32_e32 v64, v220, v64\n"
    "v_mul_f32_e32 v65, v221, v65\n"
    "v_mul_f32_e32 v66, v222, v66\n"
    "v_mul_f32_e32 v67, v223, v67\n"
    "v_mfma_f32_16x16x32_bf16 a[80:83], v[136:139], v[192:195], a[80:83]\n"
    "buffer_store_dwordx4 v[4:7], v246, s[48:51], s59 offen offset:2048 nt\n"
    "v_mul_f32_e32 v68, v220, v68\n"
    "v_mul_f32_e32 v69, v221, v69\n"
    "v_mul_f32_e32 v70, v222, v70\n"
    "v_mul_f32_e32 v71, v223, v71\n"
    "v_mul_f32_e32 v72, v220, v72\n"
    "v_mfma_f32_16x16x32_bf16 a[80:83], v[140:143], v[196:199], a[80:83]\n"
    "s_mov_b32 s59, 4096\n"
    "buffer_store_dwordx4 v[8:11], v246, s[48:51], s59 offen offset:0 nt\n"
    "v_mul_f32_e32 v73, v221, v73\n"
    "v_mul_f32_e32 v74, v222, v74\n"
    "v_mul_f32_e32 v75, v223, v75\n"
    "v_mul_f32_e32 v76, v220, v76\n"
    "v_mul_f32_e32 v77, v221, v77\n"
    "v_mfma_f32_16x16x32_bf16 a[92:95], v[140:143], v[204:207], a[92:95]\n"
    "buffer_store_dwordx4 v[12:15], v246, s[48:51], s59 offen offset:2048 nt\n"
    "v_mul_f32_e32 v78, v222, v78\n"
    "v_mul_f32_e32 v79, v223, v79\n"
    "v_cvt_pk_bf16_f32 v98, v64, v68\n"
    "v_cvt_pk_bf16_f32 v99, v72, v76\n"
    "v_cvt_pk_bf16_f32 v102, v65, v69\n"
    "v_mfma_f32_16x16x32_bf16 a[92:95], v[136:139], v[200:203], a[92:95]\n"
    "s_waitcnt lgkmcnt(0)\n"
    "v_cvt_pk_bf16_f32 v103, v73, v77\n"
    "v_cvt_pk_bf16_f32 v106, v66, v70\n"
    "v_cvt_pk_bf16_f32 v107, v74, v78\n"
    "v_cvt_pk_bf16_f32 v110, v67, v71\n"
    "v_cvt_pk_bf16_f32 v111, v75, v79\n"
    "v_mfma_f32_16x16x32_bf16 a[96:99], v[120:123], a[192:195], a[96:99]\n"
    "ds_write_b128 v232, v[96:99] offset:4096\n"
    "ds_write_b128 v232, v[100:103] offset:4352\n"
    "v_accvgpr_read_b32 v48, a8\n"
    "v_accvgpr_read_b32 v49, a9\n"
    "v_accvgpr_read_b32 v50, a10\n"
    "v_accvgpr_read_b32 v51, a11\n"
    "v_accvgpr_read_b32 v52, a20\n"
    "v_mfma_f32_16x16x32_bf16 a[96:99], v[124:127], a[196:199], a[96:99]\n"
    "ds_write_b128 v232, v[104:107] offset:4608\n"
    "ds_write_b128 v232, v[108:111] offset:4864\n"
    "v_accvgpr_read_b32 v53, a21\n"
    "v_accvgpr_read_b32 v54, a22\n"
    "v_accvgpr_read_b32 v55, a23\n"
    "v_accvgpr_read_b32 v56, a32\n"
    "v_accvgpr_read_b32 v57, a33\n"
    "v_mfma_f32_16x16x32_bf16 a[108:111], v[124:127], a[204:207], a[108:111]\n"
    "v_accvgpr_read_b32 v58, a34\n"
    "v_accvgpr_read_b32 v59, a35\n"
    "v_accvgpr_read_b32 v60, a44\n"
    "v_accvgpr_read_b32 v61, a45\n"
    "v_accvgpr_read_b32 v62, a46\n"
    "v_mfma_f32_16x16x32_bf16 a[108:111], v[120:123], a[200:203], a[108:111]\n"
    "v_accvgpr_read_b32 v63, a47\n"
    "v_mul_f32_e32 v48, v224, v48\n"
    "v_mul_f32_e32 v49, v225, v49\n"
    "v_mul_f32_e32 v50, v226, v50\n"
    "v_mul_f32_e32 v51, v227, v51\n"
    "v_mfma_f32_16x16x32_bf16 a[120:123], v[120:123], a[208:211], a[120:123]\n"
    "s_waitcnt lgkmcnt(0)\n"
    "ds_read_b64_tr_b16 v[16:17], v233 offset:4096\n"
    "ds_read_b64_tr_b16 v[18:19], v233 offset:4160\n"
    "v_mul_f32_e32 v52, v224, v52\n"
    "v_mul_f32_e32 v53, v225, v53\n"
    "v_mul_f32_e32 v54, v226, v54\n"
    "v_mul_f32_e32 v55, v227, v55\n"
    "v_mul_f32_e32 v56, v224, v56\n"
    "v_mfma_f32_16x16x32_bf16 a[120:123], v[124:127], a[212:215], a[120:123]\n"
    "ds_read_b64_tr_b16 v[20:21], v233 offset:5120\n"
    "ds_read_b64_tr_b16 v[22:23], v233 offset:5184\n"
    "v_mul_f32_e32 v57, v225, v57\n"
    "v_mul_f32_e32 v58, v226, v58\n"
    "v_mul_f32_e32 v59, v227, v59\n"
    "v_mul_f32_e32 v60, v224, v60\n"
    "v_mul_f32_e32 v61, v225, v61\n"
    "v_mfma_f32_16x16x32_bf16 a[132:135], v[124:127], a[220:223], a[132:135]\n"
    "ds_read_b64_tr_b16 v[24:25], v233 offset:6144\n"
    "ds_read_b64_tr_b16 v[26:27], v233 offset:6208\n"
    "v_mul_f32_e32 v62, v226, v62\n"
    "v_mul_f32_e32 v63, v227, v63\n"
    "v_cvt_pk_bf16_f32 v80, v48, v52\n"
    "v_cvt_pk_bf16_f32 v81, v56, v60\n"
    "v_cvt_pk_bf16_f32 v84, v49, v53\n"
    "v_mfma_f32_16x16x32_bf16 a[132:135], v[120:123], a[216:219], a[132:135]\n"
    "ds_read_b64_tr_b16 v[28:29], v233 offset:7168\n"
    "ds_read_b64_tr_b16 v[30:31], v233 offset:7232\n"
    "v_cvt_pk_bf16_f32 v85, v57, v61\n"
    "v_cvt_pk_bf16_f32 v88, v50, v54\n"
    "v_cvt_pk_bf16_f32 v89, v58, v62\n"
    "v_cvt_pk_bf16_f32 v92, v51, v55\n"
    "v_cvt_pk_bf16_f32 v93, v59, v63\n"
    "v_mfma_f32_16x16x32_bf16 a[144:147], v[120:123], a[224:227], a[144:147]\n"
    "v_accvgpr_read_b32 v64, a56\n"
    "v_accvgpr_read_b32 v65, a57\n"
    "v_accvgpr_read_b32 v66, a58\n"
    "v_accvgpr_read_b32 v67, a59\n"
    "v_accvgpr_read_b32 v68, a68\n"
    "v_mfma_f32_16x16x32_bf16 a[144:147], v[124:127], a[228:231], a[144:147]\n"
    "v_accvgpr_read_b32 v69, a69\n"
    "v_accvgpr_read_b32 v70, a70\n"
    "v_accvgpr_read_b32 v71, a71\n"
    "v_accvgpr_read_b32 v72, a80\n"
    "v_accvgpr_read_b32 v73, a81\n"
    "v_mfma_f32_16x16x32_bf16 a[156:159], v[124:127], a[236:239], a[156:159]\n"
    "v_accvgpr_read_b32 v74, a82\n"
    "v_accvgpr_read_b32 v75, a83\n"
    "v_accvgpr_read_b32 v76, a92\n"
    "v_accvgpr_read_b32 v77, a93\n"
    "v_accvgpr_read_b32 v78, a94\n"
    "v_mfma_f32_16x16x32_bf16 a[156:159], v[120:123], a[232:235], a[156:159]\n"
    "s_waitcnt lgkmcnt(0)\n"
    "s_mov_b32 s59, 8192\n"
    "buffer_store_dwordx4 v[16:19], v246, s[48:51], s59 offen offset:0 nt\n"
    "v_accvgpr_read_b32 v79, a95\n"
    "v_mul_f32_e32 v64, v224, v64\n"
    "v_mul_f32_e32 v65, v225, v65\n"
    "v_mul_f32_e32 v66, v226, v66\n"
    "v_mul_f32_e32 v67, v227, v67\n"
    "v_mfma_f32_16x16x32_bf16 a[168:171], v[120:123], a[240:243], a[168:171]\n"
    "buffer_store_dwordx4 v[20:23], v246, s[48:51], s59 offen offset:2048 nt\n"
    "v_mul_f32_e32 v68, v224, v68\n"
    "v_mul_f32_e32 v69, v225, v69\n"
    "v_mul_f32_e32 v70, v226, v70\n"
    "v_mul_f32_e32 v71, v227, v71\n"
    "v_mul_f32_e32 v72, v224, v72\n"
    "v_mfma_f32_16x16x32_bf16 a[168:171], v[124:127], a[244:247], a[168:171]\n"
    "s_mov_b32 s59, 12288\n"
    "buffer_store_dwordx4 v[24:27], v246, s[48:51], s59 offen offset:0 nt\n"
    "v_mul_f32_e32 v73, v225, v73\n"
    "v_mul_f32_e32 v74, v226, v74\n"
    "v_mul_f32_e32 v75, v227, v75\n"
    "v_mul_f32_e32 v76, v224, v76\n"
    "v_mul_f32_e32 v77, v225, v77\n"
    "v_mfma_f32_16x16x32_bf16 a[180:183], v[124:127], a[252:255], a[180:183]\n"
    "buffer_store_dwordx4 v[28:31], v246, s[48:51], s59 offen offset:2048 nt\n"
    "v_mul_f32_e32 v78, v226, v78\n"
    "v_mul_f32_e32 v79, v227, v79\n"
    "v_cvt_pk_bf16_f32 v82, v64, v68\n"
    "v_cvt_pk_bf16_f32 v83, v72, v76\n"
    "v_cvt_pk_bf16_f32 v86, v65, v69\n"
    "v_mfma_f32_16x16x32_bf16 a[180:183], v[120:123], a[248:251], a[180:183]\n"
    "v_cvt_pk_bf16_f32 v87, v73, v77\n"
    "v_cvt_pk_bf16_f32 v90, v66, v70\n"
    "v_cvt_pk_bf16_f32 v91, v74, v78\n"
    "v_cvt_pk_bf16_f32 v94, v67, v71\n"
    "v_cvt_pk_bf16_f32 v95, v75, v79\n"
    "v_mfma_f32_16x16x32_bf16 a[100:103], v[128:131], a[192:195], a[100:103]\n"
    "ds_write_b128 v232, v[80:83] offset:8192\n"
    "ds_write_b128 v232, v[84:87] offset:8448\n"
    "v_accvgpr_read_b32 v48, a96\n"
    "v_accvgpr_read_b32 v49, a97\n"
    "v_accvgpr_read_b32 v50, a98\n"
    "v_accvgpr_read_b32 v51, a99\n"
    "v_accvgpr_read_b32 v52, a108\n"
    "v_mfma_f32_16x16x32_bf16 a[100:103], v[132:135], a[196:199], a[100:103]\n"
    "ds_write_b128 v232, v[88:91] offset:8704\n"
    "ds_write_b128 v232, v[92:95] offset:8960\n"
    "v_accvgpr_read_b32 v53, a109\n"
    "v_accvgpr_read_b32 v54, a110\n"
    "v_accvgpr_read_b32 v55, a111\n"
    "v_accvgpr_read_b32 v56, a120\n"
    "v_accvgpr_read_b32 v57, a121\n"
    "v_mfma_f32_16x16x32_bf16 a[112:115], v[132:135], a[204:207], a[112:115]\n"
    "v_accvgpr_read_b32 v58, a122\n"
    "v_accvgpr_read_b32 v59, a123\n"
    "v_accvgpr_read_b32 v60, a132\n"
    "v_accvgpr_read_b32 v61, a133\n"
    "v_accvgpr_read_b32 v62, a134\n"
    "v_mfma_f32_16x16x32_bf16 a[112:115], v[128:131], a[200:203], a[112:115]\n"
    "v_accvgpr_read_b32 v63, a135\n"
    "v_mul_f32_e32 v48, v216, v48\n"
    "v_mul_f32_e32 v49, v217, v49\n"
    "v_mul_f32_e32 v50, v218, v50\n"
    "v_mul_f32_e32 v51, v219, v51\n"
    "v_mfma_f32_16x16x32_bf16 a[124:127], v[128:131], a[208:211], a[124:127]\n"
    "s_waitcnt lgkmcnt(0)\n"
    "ds_read_b64_tr_b16 v[0:1], v233 offset:8192\n"
    "ds_read_b64_tr_b16 v[2:3], v233 offset:8256\n"
    "v_mul_f32_e32 v52, v216, v52\n"
    "v_mul_f32_e32 v53, v217, v53\n"
    "v_mul_f32_e32 v54, v218, v54\n"
    "v_mul_f32_e32 v55, v219, v55\n"
    "v_mul_f32_e32 v56, v216, v56\n"
    "v_mfma_f32_16x16x32_bf16 a[124:127], v[132:135], a[212:215], a[124:127]\n"
    "ds_read_b64_tr_b16 v[4:5], v233 offset:9216\n"
    "ds_read_b64_tr_b16 v[6:7], v233 offset:9280\n"
    "v_mul_f32_e32 v57, v217, v57\n"
    "v_mul_f32_e32 v58, v218, v58\n"
    "v_mul_f32_e32 v59, v219, v59\n"
    "v_mul_f32_e32 v60, v216, v60\n"
    "v_mul_f32_e32 v61, v217, v61\n"
    "v_mfma_f32_16x16x32_bf16 a[136:139], v[132:135], a[220:223], a[136:139]\n"
    "ds_read_b64_tr_b16 v[8:9], v233 offset:10240\n"
    "ds_read_b64_tr_b16 v[10:11], v233 offset:10304\n"
    "v_mul_f32_e32 v62, v218, v62\n"
    "v_mul_f32_e32 v63, v219, v63\n"
    "v_cvt_pk_bf16_f32 v96, v48, v52\n"
    "v_cvt_pk_bf16_f32 v97, v56, v60\n"
    "v_cvt_pk_bf16_f32 v100, v49, v53\n"
    "v_mfma_f32_16x16x32_bf16 a[136:139], v[128:131], a[216:219], a[136:139]\n"
    "ds_read_b64_tr_b16 v[12:13], v233 offset:11264\n"
    "ds_read_b64_tr_b16 v[14:15], v233 offset:11328\n"
    "v_cvt_pk_bf16_f32 v101, v57, v61\n"
    "v_cvt_pk_bf16_f32 v104, v50, v54\n"
    "v_cvt_pk_bf16_f32 v105, v58, v62\n"
    "v_cvt_pk_bf16_f32 v108, v51, v55\n"
    "v_cvt_pk_bf16_f32 v109, v59, v63\n"
    "v_mfma_f32_16x16x32_bf16 a[148:151], v[128:131], a[224:227], a[148:151]\n"
    "v_accvgpr_read_b32 v64, a144\n"
    "v_accvgpr_read_b32 v65, a145\n"
    "v_accvgpr_read_b32 v66, a146\n"
    "v_accvgpr_read_b32 v67, a147\n"
    "v_accvgpr_read_b32 v68, a156\n"
    "v_mfma_f32_16x16x32_bf16 a[148:151], v[132:135], a[228:231], a[148:151]\n"
    "v_accvgpr_read_b32 v69, a157\n"
    "v_accvgpr_read_b32 v70, a158\n"
    "v_accvgpr_read_b32 v71, a159\n"
    "v_accvgpr_read_b32 v72, a168\n"
    "v_accvgpr_read_b32 v73, a169\n"
    "v_mfma_f32_16x16x32_bf16 a[160:163], v[132:135], a[236:239], a[160:163]\n"
    "v_accvgpr_read_b32 v74, a170\n"
    "v_accvgpr_read_b32 v75, a171\n"
    "v_accvgpr_read_b32 v76, a180\n"
    "v_accvgpr_read_b32 v77, a181\n"
    "v_accvgpr_read_b32 v78, a182\n"
    "v_mfma_f32_16x16x32_bf16 a[160:163], v[128:131], a[232:235], a[160:163]\n"
    "s_waitcnt lgkmcnt(0)\n"
    "s_mov_b32 s59, 16384\n"
    "buffer_store_dwordx4 v[0:3], v246, s[48:51], s59 offen offset:0 nt\n"
    "v_accvgpr_read_b32 v79, a183\n"
    "v_mul_f32_e32 v64, v216, v64\n"
    "v_mul_f32_e32 v65, v217, v65\n"
    "v_mul_f32_e32 v66, v218, v66\n"
    "v_mul_f32_e32 v67, v219, v67\n"
    "v_mfma_f32_16x16x32_bf16 a[172:175], v[128:131], a[240:243], a[172:175]\n"
    "buffer_store_dwordx4 v[4:7], v246, s[48:51], s59 offen offset:2048 nt\n"
    "v_mul_f32_e32 v68, v216, v68\n"
    "v_mul_f32_e32 v69, v217, v69\n"
    "v_mul_f32_e32 v70, v218, v70\n"
    "v_mul_f32_e32 v71, v219, v71\n"
    "v_mul_f32_e32 v72, v216, v72\n"
    "v_mfma_f32_16x16x32_bf16 a[172:175], v[132:135], a[244:247], a[172:175]\n"
    "s_mov_b32 s59, 20480\n"
    "buffer_store_dwordx4 v[8:11], v246, s[48:51], s59 offen offset:0 nt\n"
    "v_mul_f32_e32 v73, v217, v73\n"
    "v_mul_f32_e32 v74, v218, v74\n"
    "v_mul_f32_e32 v75, v219, v75\n"
    "v_mul_f32_e32 v76, v216, v76\n"
    "v_mul_f32_e32 v77, v217, v77\n"
    "v_mfma_f32_16x16x32_bf16 a[184:187], v[132:135], a[252:255], a[184:187]\n"
    "buffer_store_dwordx4 v[12:15], v246, s[48:51], s59 offen offset:2048 nt\n"
    "v_mul_f32_e32 v78, v218, v78\n"
    "v_mul_f32_e32 v79, v219, v79\n"
    "v_cvt_pk_bf16_f32 v98, v64, v68\n"
    "v_cvt_pk_bf16_f32 v99, v72, v76\n"
    "v_cvt_pk_bf16_f32 v102, v65, v69\n"
    "v_mfma_f32_16x16x32_bf16 a[184:187], v[128:131], a[248:251], a[184:187]\n"
    "v_cvt_pk_bf16_f32 v103, v73, v77\n"
    "v_cvt_pk_bf16_f32 v106, v66, v70\n"
    "v_cvt_pk_bf16_f32 v107, v74, v78\n"
    "v_cvt_pk_bf16_f32 v110, v67, v71\n"
    "v_cvt_pk_bf16_f32 v111, v75, v79\n"
    "v_mfma_f32_16x16x32_bf16 a[104:107], v[136:139], a[192:195], a[104:107]\n"
    "ds_write_b128 v232, v[96:99] offset:12288\n"
    "ds_write_b128 v232, v[100:103] offset:12544\n"
    "v_accvgpr_read_b32 v48, a100\n"
    "v_accvgpr_read_b32 v49, a101\n"
    "v_accvgpr_read_b32 v50, a102\n"
    "v_accvgpr_read_b32 v51, a103\n"
    "v_accvgpr_read_b32 v52, a112\n"
    "v_mfma_f32_16x16x32_bf16 a[104:107], v[140:143], a[196:199], a[104:107]\n"
    "ds_write_b128 v232, v[104:107] offset:12800\n"
    "ds_write_b128 v232, v[108:111] offset:13056\n"
    "v_accvgpr_read_b32 v53, a113\n"
    "v_accvgpr_read_b32 v54, a114\n"
    "v_accvgpr_read_b32 v55, a115\n"
    "v_accvgpr_read_b32 v56, a124\n"
    "v_accvgpr_read_b32 v57, a125\n"
    "v_mfma_f32_16x16x32_bf16 a[116:119], v[140:143], a[204:207], a[116:119]\n"
    "v_accvgpr_read_b32 v58, a126\n"
    "v_accvgpr_read_b32 v59, a127\n"
    "v_accvgpr_read_b32 v60, a136\n"
    "v_accvgpr_read_b32 v61, a137\n"
    "v_accvgpr_read_b32 v62, a138\n"
    "v_mfma_f32_16x16x32_bf16 a[116:119], v[136:139], a[200:203], a[116:119]\n"
    "v_accvgpr_read_b32 v63, a139\n"
    "v_mul_f32_e32 v48, v220, v48\n"
    "v_mul_f32_e32 v49, v221, v49\n"
    "v_mul_f32_e32 v50, v222, v50\n"
    "v_mul_f32_e32 v51, v223, v51\n"
    "v_mfma_f32_16x16x32_bf16 a[128:131], v[136:139], a[208:211], a[128:131]\n"
    "s_waitcnt lgkmcnt(0)\n"
    "ds_read_b64_tr_b16 v[16:17], v233 offset:12288\n"
    "ds_read_b64_tr_b16 v[18:19], v233 offset:12352\n"
    "v_mul_f32_e32 v52, v220, v52\n"
    "v_mul_f32_e32 v53, v221, v53\n"
    "v_mul_f32_e32 v54, v222, v54\n"
    "v_mul_f32_e32 v55, v223, v55\n"
    "v_mul_f32_e32 v56, v220, v56\n"
    "v_mfma_f32_16x16x32_bf16 a[128:131], v[140:143], a[212:215], a[128:131]\n"
    "ds_read_b64_tr_b16 v[20:21], v233 offset:13312\n"
    "ds_read_b64_tr_b16 v[22:23], v233 offset:13376\n"
    "v_mul_f32_e32 v57, v221, v57\n"
    "v_mul_f32_e32 v58, v222, v58\n"
    "v_mul_f32_e32 v59, v223, v59\n"
    "v_mul_f32_e32 v60, v220, v60\n"
    "v_mul_f32_e32 v61, v221, v61\n"
    "v_mfma_f32_16x16x32_bf16 a[140:143], v[140:143], a[220:223], a[140:143]\n"
    "ds_read_b64_tr_b16 v[24:25], v233 offset:14336\n"
    "ds_read_b64_tr_b16 v[26:27], v233 offset:14400\n"
    "v_mul_f32_e32 v62, v222, v62\n"
    "v_mul_f32_e32 v63, v223, v63\n"
    "v_cvt_pk_bf16_f32 v80, v48, v52\n"
    "v_cvt_pk_bf16_f32 v81, v56, v60\n"
    "v_cvt_pk_bf16_f32 v84, v49, v53\n"
    "v_mfma_f32_16x16x32_bf16 a[140:143], v[136:139], a[216:219], a[140:143]\n"
    "ds_read_b64_tr_b16 v[28:29], v233 offset:15360\n"
    "ds_read_b64_tr_b16 v[30:31], v233 offset:15424\n"
    "v_cvt_pk_bf16_f32 v85, v57, v61\n"
    "v_cvt_pk_bf16_f32 v88, v50, v54\n"
    "v_cvt_pk_bf16_f32 v89, v58, v62\n"
    "v_cvt_pk_bf16_f32 v92, v51, v55\n"
    "v_cvt_pk_bf16_f32 v93, v59, v63\n"
    "v_mfma_f32_16x16x32_bf16 a[152:155], v[136:139], a[224:227], a[152:155]\n"
    "v_accvgpr_read_b32 v64, a148\n"
    "v_accvgpr_read_b32 v65, a149\n"
    "v_accvgpr_read_b32 v66, a150\n"
    "v_accvgpr_read_b32 v67, a151\n"
    "v_accvgpr_read_b32 v68, a160\n"
    "v_mfma_f32_16x16x32_bf16 a[152:155], v[140:143], a[228:231], a[152:155]\n"
    "v_accvgpr_read_b32 v69, a161\n"
    "v_accvgpr_read_b32 v70, a162\n"
    "v_accvgpr_read_b32 v71, a163\n"
    "v_accvgpr_read_b32 v72, a172\n"
    "v_accvgpr_read_b32 v73, a173\n"
    "v_mfma_f32_16x16x32_bf16 a[164:167], v[140:143], a[236:239], a[164:167]\n"
    "v_accvgpr_read_b32 v74, a174\n"
    "v_accvgpr_read_b32 v75, a175\n"
    "v_accvgpr_read_b32 v76, a184\n"
    "v_accvgpr_read_b32 v77, a185\n"
    "v_accvgpr_read_b32 v78, a186\n"
    "v_mfma_f32_16x16x32_bf16 a[164:167], v[136:139], a[232:235], a[164:167]\n"
    "s_waitcnt lgkmcnt(0)\n"
    "s_mov_b32 s59, 0\n"
    "buffer_store_dwordx4 v[16:19], v246, s[48:51], s59 offen offset:256 nt\n"
    "v_accvgpr_read_b32 v79, a187\n"
    "v_mul_f32_e32 v64, v220, v64\n"
    "v_mul_f32_e32 v65, v221, v65\n"
    "v_mul_f32_e32 v66, v222, v66\n"
    "v_mul_f32_e32 v67, v223, v67\n"
    "v_mfma_f32_16x16x32_bf16 a[176:179], v[136:139], a[240:243], a[176:179]\n"
    "buffer_store_dwordx4 v[20:23], v246, s[48:51], s59 offen offset:2304 nt\n"
    "v_mul_f32_e32 v68, v220, v68\n"
    "v_mul_f32_e32 v69, v221, v69\n"
    "v_mul_f32_e32 v70, v222, v70\n"
    "v_mul_f32_e32 v71, v223, v71\n"
    "v_mul_f32_e32 v72, v220, v72\n"
    "v_mfma_f32_16x16x32_bf16 a[176:179], v[140:143], a[244:247], a[176:179]\n"
    "s_mov_b32 s59, 4096\n"
    "buffer_store_dwordx4 v[24:27], v246, s[48:51], s59 offen offset:256 nt\n"
    "v_mul_f32_e32 v73, v221, v73\n"
    "v_mul_f32_e32 v74, v222, v74\n"
    "v_mul_f32_e32 v75, v223, v75\n"
    "v_mul_f32_e32 v76, v220, v76\n"
    "v_mul_f32_e32 v77, v221, v77\n"
    "v_mfma_f32_16x16x32_bf16 a[188:191], v[140:143], a[252:255], a[188:191]\n"
    "buffer_store_dwordx4 v[28:31], v246, s[48:51], s59 offen offset:2304 nt\n"
    "v_mul_f32_e32 v78, v222, v78\n"
    "v_mul_f32_e32 v79, v223, v79\n"
    "v_cvt_pk_bf16_f32 v82, v64, v68\n"
    "v_cvt_pk_bf16_f32 v83, v72, v76\n"
    "v_cvt_pk_bf16_f32 v86, v65, v69\n"
    "v_mfma_f32_16x16x32_bf16 a[188:191], v[136:139], a[248:251], a[188:191]\n"
    "v_cvt_pk_bf16_f32 v87, v73, v77\n"
    "v_cvt_pk_bf16_f32 v90, v66, v70\n"
    "v_cvt_pk_bf16_f32 v91, v74, v78\n"
    "v_cvt_pk_bf16_f32 v94, v67, v71\n"
    "v_cvt_pk_bf16_f32 v95, v75, v79\n"
    "ds_write_b128 v232, v[80:83] offset:0\n"
    "ds_write_b128 v232, v[84:87] offset:256\n"
    "ds_write_b128 v232, v[88:91] offset:512\n"
    "ds_write_b128 v232, v[92:95] offset:768\n"
    "v_accvgpr_read_b32 v48, a104\n"
    "v_accvgpr_read_b32 v49, a105\n"
    "v_accvgpr_read_b32 v50, a106\n"
    "v_accvgpr_read_b32 v51, a107\n"
    "v_accvgpr_read_b32 v52, a116\n"
    "v_accvgpr_read_b32 v53, a117\n"
    "v_accvgpr_read_b32 v54, a118\n"
    "v_accvgpr_read_b32 v55, a119\n"
    "v_accvgpr_read_b32 v56, a128\n"
    "v_accvgpr_read_b32 v57, a129\n"
    "v_accvgpr_read_b32 v58, a130\n"
    "v_accvgpr_read_b32 v59, a131\n"
    "v_accvgpr_read_b32 v60, a140\n"
    "v_accvgpr_read_b32 v61, a141\n"
    "v_accvgpr_read_b32 v62, a142\n"
    "v_accvgpr_read_b32 v63, a143\n"
    "v_pk_mul_f32 v[48:49], v[48:49], v[224:225]\n"
    "v_pk_mul_f32 v[50:51], v[50:51], v[226:227]\n"
    "v_pk_mul_f32 v[52:53], v[52:53], v[224:225]\n"
    "v_pk_mul_f32 v[54:55], v[54:55], v[226:227]\n"
    "v_pk_mul_f32 v[56:57], v[56:57], v[224:225]\n"
    "v_pk_mul_f32 v[58:59], v[58:59], v[226:227]\n"
    "v_pk_mul_f32 v[60:61], v[60:61], v[224:225]\n"
    "v_pk_mul_f32 v[62:63], v[62:63], v[226:227]\n"
    "s_waitcnt lgkmcnt(0)\n"
    "ds_read_b64_tr_b16 v[0:1], v233 offset:0\n"
    "ds_read_b64_tr_b16 v[2:3], v233 offset:64\n"
    "ds_read_b64_tr_b16 v[4:5], v233 offset:1024\n"
    "ds_read_b64_tr_b16 v[6:7], v233 offset:1088\n"
    "ds_read_b64_tr_b16 v[8:9], v233 offset:2048\n"
    "ds_read_b64_tr_b16 v[10:11], v233 offset:2112\n"
    "ds_read_b64_tr_b16 v[12:13], v233 offset:3072\n"
    "ds_read_b64_tr_b16 v[14:15], v233 offset:3136\n"
    "v_cvt_pk_bf16_f32 v96, v48, v52\n"
    "v_cvt_pk_bf16_f32 v97, v56, v60\n"
    "v_cvt_pk_bf16_f32 v100, v49, v53\n"
    "v_cvt_pk_bf16_f32 v101, v57, v61\n"
    "v_cvt_pk_bf16_f32 v104, v50, v54\n"
    "v_cvt_pk_bf16_f32 v105, v58, v62\n"
    "v_cvt_pk_bf16_f32 v108, v51, v55\n"
    "v_cvt_pk_bf16_f32 v109, v59, v63\n"
    "v_accvgpr_read_b32 v64, a152\n"
    "v_accvgpr_read_b32 v65, a153\n"
    "v_accvgpr_read_b32 v66, a154\n"
    "v_accvgpr_read_b32 v67, a155\n"
    "v_accvgpr_read_b32 v68, a164\n"
    "v_accvgpr_read_b32 v69, a165\n"
    "v_accvgpr_read_b32 v70, a166\n"
    "v_accvgpr_read_b32 v71, a167\n"
    "v_accvgpr_read_b32 v72, a176\n"
    "v_accvgpr_read_b32 v73, a177\n"
    "v_accvgpr_read_b32 v74, a178\n"
    "v_accvgpr_read_b32 v75, a179\n"
    "v_accvgpr_read_b32 v76, a188\n"
    "v_accvgpr_read_b32 v77, a189\n"
    "v_accvgpr_read_b32 v78, a190\n"
    "v_accvgpr_read_b32 v79, a191\n"
    "s_waitcnt lgkmcnt(0)\n"
    "s_mov_b32 s59, 8192\n"
    "buffer_store_dwordx4 v[0:3], v246, s[48:51], s59 offen offset:256 nt\n"
    "buffer_store_dwordx4 v[4:7], v246, s[48:51], s59 offen offset:2304 nt\n"
    "s_mov_b32 s59, 12288\n"
    "buffer_store_dwordx4 v[8:11], v246, s[48:51], s59 offen offset:256 nt\n"
    "buffer_store_dwordx4 v[12:15], v246, s[48:51], s59 offen offset:2304 nt\n"
    "v_pk_mul_f32 v[64:65], v[64:65], v[224:225]\n"
    "v_pk_mul_f32 v[66:67], v[66:67], v[226:227]\n"
    "v_pk_mul_f32 v[68:69], v[68:69], v[224:225]\n"
    "v_pk_mul_f32 v[70:71], v[70:71], v[226:227]\n"
    "v_pk_mul_f32 v[72:73], v[72:73], v[224:225]\n"
    "v_pk_mul_f32 v[74:75], v[74:75], v[226:227]\n"
    "v_pk_mul_f32 v[76:77], v[76:77], v[224:225]\n"
    "v_pk_mul_f32 v[78:79], v[78:79], v[226:227]\n"
    "v_cvt_pk_bf16_f32 v98, v64, v68\n"
    "v_cvt_pk_bf16_f32 v99, v72, v76\n"
    "v_cvt_pk_bf16_f32 v102, v65, v69\n"
    "v_cvt_pk_bf16_f32 v103, v73, v77\n"
    "v_cvt_pk_bf16_f32 v106, v66, v70\n"
    "v_cvt_pk_bf16_f32 v107, v74, v78\n"
    "v_cvt_pk_bf16_f32 v110, v67, v71\n"
    "v_cvt_pk_bf16_f32 v111, v75, v79\n"
    "s_nop 0\n"
    "ds_write_b128 v232, v[96:99] offset:4096\n"
    "ds_write_b128 v232, v[100:103] offset:4352\n"
    "ds_write_b128 v232, v[104:107] offset:4608\n"
    "ds_write_b128 v232, v[108:111] offset:4864\n"
    "s_waitcnt lgkmcnt(0)\n"
    "ds_read_b64_tr_b16 v[16:17], v233 offset:4096\n"
    "ds_read_b64_tr_b16 v[18:19], v233 offset:4160\n"
    "ds_read_b64_tr_b16 v[20:21], v233 offset:5120\n"
    "ds_read_b64_tr_b16 v[22:23], v233 offset:5184\n"
    "ds_read_b64_tr_b16 v[24:25], v233 offset:6144\n"
    "ds_read_b64_tr_b16 v[26:27], v233 offset:6208\n"
    "ds_read_b64_tr_b16 v[28:29], v233 offset:7168\n"
    "ds_read_b64_tr_b16 v[30:31], v233 offset:7232\n"
    "s_waitcnt lgkmcnt(4)\n"
    "s_mov_b32 s59, 16384\n"
    "buffer_store_dwordx4 v[16:19], v246, s[48:51], s59 offen offset:256 nt\n"
    "buffer_store_dwordx4 v[20:23], v246, s[48:51], s59 offen offset:2304 nt\n"
    "s_waitcnt lgkmcnt(0)\n"
    "s_mov_b32 s59, 20480\n"
    "buffer_store_dwordx4 v[24:27], v246, s[48:51], s59 offen offset:256 nt\n"
    "buffer_store_dwordx4 v[28:31], v246, s[48:51], s59 offen offset:2304 nt\n"
    : [tid] "+v"(tid)
    : [ka] "s"(__builtin_amdgcn_kernarg_segment_ptr()), [bid] "s"(blockIdx.x)
    : "memory", "scc", "vcc",
      "v1", "v2", "v3", "v4", "v5", "v6", "v7", "v8", "v9", "v10", "v11", "v12", "v13", "v14", "v15", "v16", "v17", "v18", "v19", "v20", "v21", "v22", "v23", "v24", "v25", "v26", "v27",
      "v28", "v29", "v30", "v31", "v32", "v33", "v34", "v35", "v36", "v37", "v38", "v39", "v40", "v41", "v42", "v43", "v44", "v45", "v46", "v47", "v48", "v49", "v50", "v51", "v52",
      "v53", "v54", "v55", "v56", "v57", "v58", "v59", "v60", "v61", "v62", "v63", "v64", "v65", "v66", "v67", "v68", "v69", "v70", "v71", "v72", "v73", "v74", "v75", "v76", "v77",
      "v78", "v79", "v80", "v81", "v82", "v83", "v84", "v85", "v86", "v87", "v88", "v89", "v90", "v91", "v92", "v93", "v94", "v95", "v96", "v97", "v98", "v99", "v100", "v101", "v102",
      "v103", "v104", "v105", "v106", "v107", "v108", "v109", "v110", "v111", "v112", "v113", "v114", "v115", "v116", "v117", "v118", "v119", "v120", "v121", "v122", "v123", "v124",
      "v125", "v126", "v127", "v128", "v129", "v130", "v131", "v132", "v133", "v134", "v135", "v136", "v137", "v138", "v139", "v140", "v141", "v142", "v143", "v144", "v145", "v146",
      "v147", "v148", "v149", "v150", "v151", "v152", "v153", "v154", "v155", "v156", "v157", "v158", "v159", "v160", "v161", "v162", "v163", "v164", "v165", "v166", "v167", "v168",
      "v169", "v170", "v171", "v172", "v173", "v174", "v175", "v176", "v177", "v178", "v179", "v180", "v181", "v182", "v183", "v184", "v185", "v186", "v187", "v188", "v189", "v190",
      "v191", "v192", "v193", "v194", "v195", "v196", "v197", "v198", "v199", "v200", "v201", "v202", "v203", "v204", "v205", "v206", "v207", "v208", "v209", "v210", "v211", "v212",
      "v213", "v214", "v215", "v216", "v217", "v218", "v219", "v220", "v221", "v222", "v223", "v224", "v225", "v226", "v227", "v228", "v229", "v230", "v231", "v232", "v233", "v234",
      "v235", "v236", "v237", "v238", "v239", "v240", "v241", "v242", "v243", "v244", "v245", "v246", "v247", "v248", "v249", "v250", "v251", "v252", "v253", "v254", "v255", "a0", "a1",
      "a2", "a3", "a4", "a5", "a6", "a7", "a8", "a9", "a10", "a11", "a12", "a13", "a14", "a15", "a16", "a17", "a18", "a19", "a20", "a21", "a22", "a23", "a24", "a25", "a26", "a27",
      "a28", "a29", "a30", "a31", "a32", "a33", "a34", "a35", "a36", "a37", "a38", "a39", "a40", "a41", "a42", "a43", "a44", "a45", "a46", "a47", "a48", "a49", "a50", "a51", "a52",
      "a53", "a54", "a55", "a56", "a57", "a58", "a59", "a60", "a61", "a62", "a63", "a64", "a65", "a66", "a67", "a68", "a69", "a70", "a71", "a72", "a73", "a74", "a75", "a76", "a77",
      "a78", "a79", "a80", "a81", "a82", "a83", "a84", "a85", "a86", "a87", "a88", "a89", "a90", "a91", "a92", "a93", "a94", "a95", "a96", "a97", "a98", "a99", "a100", "a101", "a102",
      "a103", "a104", "a105", "a106", "a107", "a108", "a109", "a110", "a111", "a112", "a113", "a114", "a115", "a116", "a117", "a118", "a119", "a120", "a121", "a122", "a123", "a124",
      "a125", "a126", "a127", "a128", "a129", "a130", "a131", "a132", "a133", "a134", "a135", "a136", "a137", "a138", "a139", "a140", "a141", "a142", "a143", "a144", "a145", "a146",
      "a147", "a148", "a149", "a150", "a151", "a152", "a153", "a154", "a155", "a156", "a157", "a158", "a159", "a160", "a161", "a162", "a163", "a164", "a165", "a166", "a167", "a168",
      "a169", "a170", "a171", "a172", "a173", "a174", "a175", "a176", "a177", "a178", "a179", "a180", "a181", "a182", "a183", "a184", "a185", "a186", "a187", "a188", "a189", "a190",
      "a191", "a192", "a193", "a194", "a195", "a196", "a197", "a198", "a199", "a200", "a201", "a202", "a203", "a204", "a205", "a206", "a207", "a208", "a209", "a210", "a211", "a212",
      "a213", "a214", "a215", "a216", "a217", "a218", "a219", "a220", "a221", "a222", "a223", "a224", "a225", "a226", "a227", "a228", "a229", "a230", "a231", "a232", "a233", "a234",
      "a235", "a236", "a237", "a238", "a239", "a240", "a241", "a242", "a243", "a244", "a245", "a246", "a247", "a248", "a249", "a250", "a251", "a252", "a253", "a254", "a255", "s36",
      "s37", "s38", "s39", "s40", "s41", "s42", "s43", "s44", "s45", "s46", "s47", "s48", "s49", "s50", "s51", "s52", "s53", "s54", "s55", "s56", "s57", "s58", "s59", "s60", "s61",
      "s62", "s63", "s64", "s65", "s66", "s67", "s68", "s69", "s70", "s71", "s72", "s73", "s74", "s75", "s76", "s77", "s78", "s79", "s80", "s81", "s82", "s83", "s84", "s85", "s86",
      "s87", "s88", "s89", "s90", "s91");
}

extern "C" void kernel_launch(void* const* d_in, const int* in_sizes, int n_in, void* d_out, int out_size, void* d_ws, size_t ws_size,
                              hipStream_t stream) {
  attn_fwd<<<256, 256, 0, stream>>>((const char*)d_in[0], (const char*)d_in[1], (const char*)d_in[2], (char*)d_out, (char*)d_ws);
}
```
